```python
import torch

def _softmax(x, axis=-1):
    return torch.softmax(x, dim=axis)

def _gelu(x):
    return torch.nn.functional.gelu(x, approximate='tanh')

def _silu(x):
    return torch.nn.functional.silu(x)

def _sigmoid(x):
    return torch.sigmoid(x)

def _layernorm(x, axis=-1, eps=1e-05):
    m = x.mean(dim=axis, keepdim=True)
    v = x.var(dim=axis, keepdim=True, unbiased=False)
    return (x - m) / torch.sqrt(v + eps)

def _scatter_add(tgt, idx, src, axis=0):
    return tgt.clone().index_add_(axis, idx, src)

def _segment_sum(src, seg, n):
    out = torch.zeros((n,) + src.shape[1:], dtype=src.dtype)
    return out.index_add_(0, seg, src)

def _pad(x, pw):
    flat = tuple((v for pair in reversed(pw) for v in pair))
    return torch.nn.functional.pad(x, flat)
kernel_name = 'interleave_layers__memory_aug_recurrent_mem_tr__dense_activation_relu__prompt'

def setup_inputs():

    _w = lambda a: (lambda t: t.to(torch.bfloat16) if t.is_floating_point() else t)(torch.as_tensor(a))

    def _A_setup():
        B, S, Dm = (1, 2048, 640)
        H, Dh = (32, 128)
        M, seg = (8, 32)
        seg = min(seg, S)
        S = S // seg * seg
        return {'x': _w((lambda *s: torch.randn(*s, dtype=torch.bfloat16) * 0.05)(B, S, Dm)), 'mem0': _w((lambda *s: torch.randn(*s, dtype=torch.bfloat16) * 0.8)(B, M, Dm)), 'Wq': _w((lambda *s: torch.randn(*s, dtype=torch.bfloat16) * 0.8)(Dm, H, Dh) * Dm ** (-0.5)), 'Wk': _w((lambda *s: torch.randn(*s, dtype=torch.bfloat16) * 0.8)(Dm, H, Dh) * Dm ** (-0.5)), 'Wv': _w((lambda *s: torch.randn(*s, dtype=torch.bfloat16) * 0.8)(Dm, H, Dh) * Dm ** (-0.5)), 'Wo': _w((lambda *s: torch.randn(*s, dtype=torch.bfloat16) * 0.8)(H, Dh, Dm) * (H * Dh) ** (-0.5)), 'seg_len': seg}
    _A_in = _A_setup()

    def _B_setup():
        B, S, D, Dff = (1, 2048, 640, 1728)
        return {'x': _w((lambda *s: torch.randn(*s, dtype=torch.bfloat16) * 0.05)(B, S, D)), 'w1': _w((lambda *s: torch.randn(*s, dtype=torch.bfloat16) * 0.05)(D, Dff)), 'w2': _w((lambda *s: torch.randn(*s, dtype=torch.bfloat16) * 0.05)(Dff, D))}
    _B_in = _B_setup()
    return {'A_x': _A_in['x'], 'A_mem0': _A_in['mem0'], 'A_Wq': _A_in['Wq'], 'A_Wk': _A_in['Wk'], 'A_Wv': _A_in['Wv'], 'A_Wo': _A_in['Wo'], 'A_seg_len': _A_in['seg_len'], 'B_x': _B_in['x'], 'B_w1': _B_in['w1'], 'B_w2': _B_in['w2']}

def reference(A_x, A_mem0, A_Wq, A_Wk, A_Wv, A_Wo, A_seg_len, B_x, B_w1, B_w2):

    def _A_ref(x, mem0, Wq, Wk, Wv, Wo, seg_len):
        B, S, Dm = x.shape
        H, Dh = (Wq.shape[1], Wq.shape[2])
        M = mem0.shape[1]
        scale = Dh ** (-0.5)
        n_seg = S // seg_len
        L = M + seg_len
        idx = torch.arange(L)
        mask = (idx[:, None] < M) | (idx[:, None] >= idx[None, :])
        mem = mem0
        outs = []
        for s in range(n_seg):
            xs = x[:, s * seg_len:(s + 1) * seg_len, :].clone()
            h = torch.concatenate([mem, xs], axis=1)
            q = torch.einsum('btd,dhe->bhte', h, Wq)
            k = torch.einsum('btd,dhe->bhte', h, Wk)
            v = torch.einsum('btd,dhe->bhte', h, Wv)
            scores = torch.einsum('bhqe,bhke->bhqk', q, k) * scale
            scores = torch.where(mask[None, None], scores, -1000000000.0)
            p = _softmax(scores, axis=-1)
            o = torch.einsum('bhqk,bhke->bhqe', p, v)
            o = torch.einsum('bhte,hed->btd', o, Wo)
            mem = o[:, :M, :].clone()
            outs.append(o[:, M:, :])
        return torch.concatenate(outs, axis=1)

    def _B_ref(x, w1, w2):
        h = torch.einsum('bsd,df->bsf', x, w1)
        zero = torch.zeros_like(h)
        h = torch.maximum(h, zero)
        return torch.einsum('bsf,fd->bsd', h, w2)
    x = A_x
    x = x + _A_ref(_layernorm(x, axis=-1), A_mem0, A_Wq, A_Wk, A_Wv, A_Wo, A_seg_len)
    x = x + _B_ref(_layernorm(x, axis=-1), B_w1, B_w2)
    x = x + _A_ref(_layernorm(x, axis=-1), A_mem0, A_Wq, A_Wk, A_Wv, A_Wo, A_seg_len)
    x = x + _B_ref(_layernorm(x, axis=-1), B_w1, B_w2)
    return x
```

```cpp
#include <hip/hip_runtime.h>
#include <hip/hip_bf16.h>
#include <stdint.h>

#define LAS __attribute__((address_space(3)))
typedef __bf16 bf16x8 __attribute__((ext_vector_type(8)));
typedef __bf16 bf16x16 __attribute__((ext_vector_type(16)));
typedef __bf16 bf16x2 __attribute__((ext_vector_type(2)));
typedef short s16x4 __attribute__((ext_vector_type(4)));
typedef float f32x2 __attribute__((ext_vector_type(2)));
typedef float f32x4 __attribute__((ext_vector_type(4)));
typedef float f32x16 __attribute__((ext_vector_type(16)));
typedef unsigned v2u __attribute__((ext_vector_type(2)));
typedef unsigned v4u __attribute__((ext_vector_type(4)));
typedef unsigned v8u __attribute__((ext_vector_type(8)));
typedef __amdgpu_buffer_rsrc_t rsrc_t;
typedef LAS char* ldsp;

constexpr int NSEG = 64, DM = 640, DFF = 1728;
#define HINT_DT 11
#define RM 15
#define PSLEEP 3
#define PLACE 1
#define HINT_D2 32
#define HINT_DE (47 + 256 * 53)
#define HINT_DP 60
#define PREF 0
#define LATE 0
#define PP 0
#define PDEL 0
#define REBAL 0
#define PACE 6
#define LAG 10
constexpr unsigned CT_CNT = 0;
constexpr unsigned CT_OVF = 1024, CT_ABORT = 1280, CT_CAL = 2048;
constexpr unsigned CT_TAB = 4096;
constexpr unsigned T40 = 40960;
constexpr unsigned XN1_O = 1u << 20;
constexpr unsigned XA_O = XN1_O + 128 * T40;
constexpr unsigned FR_MIN = 14u << 20;
constexpr unsigned WS_END = FR_MIN + (68u << 20);
constexpr unsigned BK = 16384;
constexpr unsigned A_K = 0, A_V = 32 * BK, A_M = 64 * BK, A_Q = 96 * BK;
constexpr unsigned B_O = 0, B_P1 = 32 * BK, B_P2 = 52 * BK, B_XB = 67 * BK, B_XN2 = 72 * BK, B_H = 77 * BK;
__host__ __device__ constexpr unsigned fmap(unsigned f) { return (f >> 3) * BK + (f & 7) * 1024; }
#define STAMP(i)
#define STAMP_DECL
#define STAMP_OUT(u, cond)
static_assert(WS_END <= 268435456u, "workspace");
constexpr int RB_HINT = 80, RB_CAL = 81, RB_PG = 82, RB_N = 86;
constexpr unsigned PA_QKV = RB_PG * BK, PA_RH = (RB_PG + 2) * BK;
constexpr unsigned PB_ATT = RB_PG * BK, PB_WO = (RB_PG + 2) * BK, PB_F1 = PB_WO + 32 * 128, PB_F2 = PB_WO + 46 * 128, PB_RED = (RB_PG + 3) * BK;
static_assert(LAG <= 14, "ring depth");
constexpr unsigned SENT = 0xFFFFFFFFu;
constexpr unsigned SPIN_CAP = 1u << 18;
constexpr int LDS_BYTES = 160 * 1024;

enum { R_IDLE = 0, R_RH = 1, R_RW = 2, R_QKV = 3, R_ATT = 4, R_WO = 5, R_F1 = 6, R_F2 = 7, R_RED = 8 };

struct Params {
  const unsigned short* x; const unsigned short* mem0; const unsigned short* wq; const unsigned short* wk; const unsigned short* wv;
  const unsigned short* wo; const unsigned short* w1; const unsigned short* w2; unsigned short* out; char* ws;
  unsigned dt, d2, de, dp;
};

__device__ __forceinline__ rsrc_t mkrsrc(const void* p, unsigned bytes) { return __builtin_amdgcn_make_buffer_rsrc((void*)p, 0, bytes, 0x00020000); }
__device__ __forceinline__ v4u ld16(rsrc_t r, unsigned vo, unsigned so) { return __builtin_amdgcn_raw_buffer_load_b128(r, vo, so, 16); }
__device__ __forceinline__ unsigned ld4(rsrc_t r, unsigned vo, unsigned so) { return __builtin_amdgcn_raw_buffer_load_b32(r, vo, so, 16); }
__device__ __forceinline__ void st16(rsrc_t r, unsigned vo, unsigned so, v4u v) { __builtin_amdgcn_raw_buffer_store_b128(v, r, vo + so, 0, 16); }
__device__ __forceinline__ void st8(rsrc_t r, unsigned vo, unsigned so, v2u v) { __builtin_amdgcn_raw_buffer_store_b64(v, r, vo, so, 16); }
__device__ __forceinline__ void st4(rsrc_t r, unsigned vo, unsigned so, unsigned v) { __builtin_amdgcn_raw_buffer_store_b32(v, r, vo, so, 16); }
__device__ __forceinline__ unsigned pk2(float a, float b) { f32x2 f = {a, b}; return __builtin_bit_cast(unsigned, __builtin_convertvector(f, bf16x2)); }
__device__ __forceinline__ float blo(unsigned u) { return __uint_as_float(u << 16); }
__device__ __forceinline__ float bhi(unsigned u) { return __uint_as_float(u & 0xffff0000u); }
__device__ __forceinline__ unsigned umax(unsigned a, unsigned b) { return a > b ? a : b; }
__device__ __forceinline__ unsigned umin(unsigned a, unsigned b) { return a < b ? a : b; }
__device__ __forceinline__ unsigned umax3(unsigned a, unsigned b, unsigned c) { return umax(umax(a, b), c); }
__device__ __forceinline__ unsigned nosent(unsigned a) { return umin(a, 0xFFFFFFFEu); }
__device__ __forceinline__ bool all_lanes(bool p) { return __builtin_amdgcn_ballot_w64(p) == ~0ull; }
#define FENCE() asm volatile("" ::: "memory")
#define WAITV(n) asm volatile("s_waitcnt vmcnt(" #n ")" ::: "memory")
#define BARRIER() asm volatile("s_waitcnt lgkmcnt(0)\n\ts_barrier" ::: "memory")
#define BARRIER_V0() asm volatile("s_waitcnt vmcnt(0) lgkmcnt(0)\n\ts_barrier" ::: "memory")
#define UNI(x) __builtin_amdgcn_readfirstlane(x)
__device__ __forceinline__ void dma_sc1(rsrc_t r, unsigned vo, unsigned so, unsigned dst) {
  unsigned keep;
  asm volatile("s_mov_b32 %0, m0\n\ts_mov_b32 m0, %4\n\ts_nop 3\n\tbuffer_load_dwordx4 %1, %2, %3 offen sc1 lds\n\ts_mov_b32 m0, %0"
               : "=&s"(keep) : "v"(vo), "s"(r), "s"(so), "s"(dst) : "memory");
}
template <int N>
__device__ __forceinline__ void dma_sc1n(rsrc_t r, unsigned vo, unsigned so, unsigned dst) {
  unsigned keep;
#define DMA_HEAD "s_mov_b32 %0, m0\n\ts_mov_b32 m0, %4\n\ts_nop 3\n\tbuffer_load_dwordx4 %1, %2, %3 offen sc1 lds\n\t"
#define DMA_AT(o) "buffer_load_dwordx4 %1, %2, %3 offen offset:" #o " sc1 lds\n\t"
#define DMA_TAIL "s_mov_b32 m0, %0" : "=&s"(keep) : "v"(vo), "s"(r), "s"(so), "s"(dst) : "memory"
  if constexpr (N == 1) asm volatile(DMA_HEAD DMA_TAIL);
  else if constexpr (N == 2) asm volatile(DMA_HEAD DMA_AT(1024) DMA_TAIL);
  else if constexpr (N == 3) asm volatile(DMA_HEAD DMA_AT(1024) DMA_AT(2048) DMA_TAIL);
  else asm volatile(DMA_HEAD DMA_AT(1024) DMA_AT(2048) DMA_AT(3072) DMA_TAIL);
}
__device__ __forceinline__ void dma_pl(rsrc_t r, unsigned vo, unsigned so, unsigned dst) {
  unsigned keep;
  asm volatile("s_mov_b32 %0, m0\n\ts_mov_b32 m0, %4\n\ts_nop 3\n\tbuffer_load_dwordx4 %1, %2, %3 offen lds\n\ts_mov_b32 m0, %0"
               : "=&s"(keep) : "v"(vo), "s"(r), "s"(so), "s"(dst) : "memory");
}
template <int N> __device__ __forceinline__ void waitv() { asm volatile("s_waitcnt vmcnt(%0)" :: "i"(N) : "memory"); }
__device__ __forceinline__ v2u trread(ldsp p) { return __builtin_bit_cast(v2u, __builtin_amdgcn_ds_read_tr16_b64_v4i16((LAS s16x4*)p)); }
__device__ __forceinline__ v4u lds16(ldsp p) { return *(LAS v4u*)p; }
__device__ __forceinline__ v2u lds8(ldsp p) { return *(LAS v2u*)p; }
__device__ __forceinline__ f32x16 mfma32(v4u a, v4u b, f32x16 c) { return __builtin_amdgcn_mfma_f32_32x32x16_bf16(__builtin_bit_cast(bf16x8, a), __builtin_bit_cast(bf16x8, b), c, 0, 0, 0); }
__device__ __forceinline__ f32x4 mfma16(v4u a, v4u b, f32x4 c) { return __builtin_amdgcn_mfma_f32_16x16x32_bf16(__builtin_bit_cast(bf16x8, a), __builtin_bit_cast(bf16x8, b), c, 0, 0, 0); }
__device__ __forceinline__ f32x4 mfma16k16(v2u a, v2u b, f32x4 c) { return __builtin_amdgcn_mfma_f32_16x16x16bf16_1k(__builtin_bit_cast(s16x4, a), __builtin_bit_cast(s16x4, b), c, 0, 0, 0); }
__device__ __forceinline__ f32x4 smf(v4u a, v8u b, f32x4 c, int idx) { return __builtin_amdgcn_smfmac_f32_16x16x64_bf16(__builtin_bit_cast(bf16x8, a), __builtin_bit_cast(bf16x16, b), c, idx, 0, 0); }
__device__ __forceinline__ float ror8(float x) { return __int_as_float(__builtin_amdgcn_update_dpp(0, __float_as_int(x), 0x128, 0xf, 0xf, true)); }
__device__ __forceinline__ float sw32add(float x) { auto r = __builtin_amdgcn_permlane32_swap(__float_as_uint(x), __float_as_uint(x), false, false); return __uint_as_float(r[0]) + __uint_as_float(r[1]); }
__device__ __forceinline__ float sw32max(float x) { auto r = __builtin_amdgcn_permlane32_swap(__float_as_uint(x), __float_as_uint(x), false, false); return fmaxf(__uint_as_float(r[0]), __uint_as_float(r[1])); }
__device__ __forceinline__ float sw16add(float x) { auto r = __builtin_amdgcn_permlane16_swap(__float_as_uint(x), __float_as_uint(x), false, false); return __uint_as_float(r[0]) + __uint_as_float(r[1]); }
__device__ __forceinline__ float sw16max(float x) { auto r = __builtin_amdgcn_permlane16_swap(__float_as_uint(x), __float_as_uint(x), false, false); return fmaxf(__uint_as_float(r[0]), __uint_as_float(r[1])); }
__device__ __forceinline__ unsigned xcc_id() { return (unsigned)__builtin_amdgcn_s_getreg(20 | (3 << 11)) & 7u; }

__device__ __forceinline__ void spin_check(rsrc_t ws, unsigned& spins) {
  asm volatile("s_add_u32 %0, %0, 1\n\ts_cmp_lt_u32 %0, %2\n\ts_cbranch_scc1 1f\n\tv_mov_b32 v0, 1\n\tbuffer_store_dword v0, off, %1, 0 offset:%3 sc1\n\ts_endpgm\n1:"
               : "+s"(spins) : "s"(ws), "i"(SPIN_CAP), "i"(CT_ABORT) : "scc", "memory");
}
__device__ __forceinline__ void wait_ge(rsrc_t ws, unsigned vo, unsigned so, unsigned target, unsigned bias = 0) {
  unsigned spins = 0;
  for (;;) {
    FENCE();
    unsigned v = ld4(ws, vo, so);
    if (all_lanes(v + bias >= target)) break;
    __builtin_amdgcn_s_sleep(PSLEEP);
    spin_check(ws, spins);
  }
  FENCE();
}

#define SB() __builtin_amdgcn_sched_barrier(0)
#define PINV(x) asm volatile("" : "+v"(x))
__device__ __forceinline__ void flag_store(rsrc_t ws, unsigned so, unsigned val) {
  asm volatile("s_mov_b64 exec, 1\n\tbuffer_store_dword %0, off, %1, %2 sc1\n\ts_mov_b64 exec, -1" :: "v"(val), "s"(ws), "s"(so) : "memory");
}
__device__ __forceinline__ void ld4_asm(unsigned& d, rsrc_t ws, unsigned vo, unsigned so) {
  asm volatile("s_nop 4\n\tbuffer_load_dword %0, %1, %2, %3 offen sc1" : "+v"(d) : "v"(vo), "s"(ws), "s"(so) : "memory");
}
__device__ __forceinline__ void wait_ge2(rsrc_t ws, unsigned vo, unsigned so, unsigned tgt, unsigned& a, unsigned& b) {
  unsigned n;
  asm volatile("s_mov_b32 %2, 0\n\ts_nop 3\n\tbuffer_load_dword %0, %3, %4, %5 offen sc1\n\ts_sleep 2\n"
               "1:\n\tbuffer_load_dword %1, %3, %4, %5 offen sc1\n\ts_waitcnt vmcnt(1)\n\tv_cmp_gt_i32 vcc, %6, %0\n\ts_cbranch_vccz 2f\n\t"
               "buffer_load_dword %0, %3, %4, %5 offen sc1\n\ts_waitcnt vmcnt(1)\n\tv_cmp_gt_i32 vcc, %6, %1\n\ts_cbranch_vccz 2f\n\t"
               "s_add_u32 %2, %2, 1\n\ts_cmp_lt_u32 %2, %7\n\ts_cbranch_scc1 1b\n\t"
               "v_mov_b32 v0, 1\n\tbuffer_store_dword v0, off, %4, 0 offset:%8 sc1\n\ts_endpgm\n2:"
               : "=&v"(a), "=&v"(b), "=&s"(n) : "v"(vo), "s"(ws), "s"(so), "v"(tgt), "i"(SPIN_CAP), "i"(CT_ABORT) : "scc", "vcc", "memory");
}
__device__ __forceinline__ unsigned lds_arrive(unsigned addr) {
  unsigned r; const unsigned one = 1;
  asm volatile("s_mov_b64 exec, 1\n\tds_add_rtn_u32 %0, %1, %2\n\ts_mov_b64 exec, -1\n\ts_waitcnt lgkmcnt(0)" : "=&v"(r) : "v"(addr), "v"(one) : "memory");
  return UNI(r);
}
struct Ctx { Params p; rsrc_t ws; unsigned fb, cm, ring, ringb; int wave, lane; ldsp lds; unsigned lds0; unsigned dbg; };
__device__ __forceinline__ unsigned item(const Ctx& c, int layer, int s, unsigned side) {
  const unsigned k = layer * 16 + (s & RM); return c.fb + ((k + 1) << 21) + ((((c.cm >> k) & 1u) ^ side) << 13); }

template <int KIND, bool SWP>
__device__ __forceinline__ void gemm_role(const Ctx& c, int idx) {
  constexpr int KS = KIND == R_F2 ? 36 : 40;
  constexpr int NFR = KIND == R_WO ? 64 : (KIND == R_F2 ? 36 : 40);
  constexpr unsigned BUFB = 65536;
  const int pw = c.wave, wave = KIND == R_WO ? (0x75326410 >> (4 * pw)) & 7 : pw, lane = c.lane;
  const bool first = pw < 4;
  int layer; rsrc_t wr; unsigned wbase, ld; bool active = true;
  unsigned a_base, o_base, fso, pgo; int a_f0 = 0, fn, o_slice = 0;
  if constexpr (KIND == R_QKV) {
    layer = idx / 48; const int j = idx % 48; ld = 8192;
    const int sl = wave & 3, hf = wave >> 2;
    if (j < 32) { wr = mkrsrc(hf ? c.p.wv : c.p.wk, 640 * 8192); wbase = j * 256 + sl * 64; o_base = (hf ? A_V : A_K) + j * BK + sl * 2048; }
    else { const int hd = 2 * (j - 32) + hf; wr = mkrsrc(c.p.wq, 640 * 8192); wbase = hd * 256 + sl * 64; o_base = A_Q + hd * BK + sl * 2048; }
    a_base = XN1_O + layer * 64 * T40; fso = c.ringb + PB_RED + layer * 512; fn = 4; pgo = c.ring + PA_QKV + layer * BK + j * 128;
  } else if constexpr (KIND == R_WO) {
    layer = idx >> 4; const int kq = (idx >> 2) & 3, cg = idx & 3; ld = 1280;
    wr = mkrsrc(c.p.wo, 4096 * 1280); wbase = kq * 1024 * 1280 + cg * 320;
    a_base = B_O + kq * 8 * BK; o_slice = cg * 5 + ((5 * wave + 4) >> 3); o_base = B_P1 + kq * 5 * BK + fmap(o_slice * 2);
    fso = c.ringb + PB_ATT + layer * BK + kq * 1024; fn = 8; pgo = c.ringb + PB_WO + idx * 128;
  } else if constexpr (KIND == R_F1) {
    layer = idx / 7; const int j = idx % 7; ld = 3456; const int sl = j * 8 + wave; active = sl < 54;
    wr = mkrsrc(c.p.w1, 640 * 3456); wbase = sl * 64;
    a_base = B_XN2; o_base = B_H + fmap(sl * 2);
    fso = c.ringb + PB_RED + layer * 1024; fn = 4; pgo = c.ringb + PB_F1 + idx * 128;
  } else {
    layer = idx / 9; const int j = idx % 9, th = j / 3, cj = j % 3; ld = 1280; const int sl = cj * 7 + wave; active = wave < 7 && sl < 20;
    wr = mkrsrc(c.p.w2, 1728 * 1280); wbase = th * 576 * 1280 + sl * 64;
    a_base = B_H; a_f0 = th * 36; o_slice = sl; o_base = B_P2 + th * 5 * BK + fmap(sl * 2);
    fso = c.ringb + PB_F1 + (layer * 7 + th * 2) * 128; fn = 3; pgo = c.ringb + PB_F2 + idx * 128;
  }
  v4u W[KS];
  {
    const unsigned vw = (lane >> 2) * ld + (lane & 3) * 16, st0 = c.lds0 + pw * 16384;
    const ldsp tp = c.lds + pw * 16384 + (4 * (lane >> 5) + ((lane >> 2) & 3)) * 64 + ((lane >> 4) & 1) * 32 + (lane & 3) * 8;
    auto woff = [&](int ks) -> unsigned {
      if constexpr (KIND == R_WO) { const int gm = 5 * wave + (ks >> 3); return wbase + ((gm & 7) * 8 + (ks & 7)) * 16 * ld + (gm >> 3) * 64; }
      else return wbase + ks * 16 * ld; };
    if (active) {
#pragma unroll
      for (int ks = 0; ks < 16; ++ks) dma_pl(wr, vw, woff(ks), st0 + ks * 1024);
    }
#pragma unroll
    for (int ks = 0; ks < KS; ++ks) {
      constexpr int dummy = 0; (void)dummy;
      if (ks + 16 <= KS) waitv<15>(); else { switch (KS - 1 - ks) {
        case 14: waitv<14>(); break; case 13: waitv<13>(); break; case 12: waitv<12>(); break; case 11: waitv<11>(); break; case 10: waitv<10>(); break;
        case 9: waitv<9>(); break; case 8: waitv<8>(); break; case 7: waitv<7>(); break; case 6: waitv<6>(); break; case 5: waitv<5>(); break;
        case 4: waitv<4>(); break; case 3: waitv<3>(); break; case 2: waitv<2>(); break; case 1: waitv<1>(); break; default: waitv<0>(); } }
      v2u a = trread(tp + (ks & 15) * 1024), b = trread(tp + (ks & 15) * 1024 + 512);
      W[ks] = (v4u){a.x, a.y, b.x, b.y};
      asm volatile("s_waitcnt lgkmcnt(0)" : "+v"(W[ks]) :: "memory");
      if (ks + 16 < KS && active) dma_pl(wr, vw, woff(ks + 16), st0 + (ks & 15) * 1024);
    }
  }
  constexpr unsigned CNT = 2 * BUFB + 28672;
  if (wave == 0) *(LAS unsigned*)(c.lds + CNT + lane * 4) = 0u;
  BARRIER_V0();
  const bool back = KIND == R_QKV && (layer == 0 || (lane & 4));
  const unsigned vl = lane * 16, vf = KIND == R_QKV ? (lane & 3) * 128 + (layer && back ? 1024 : 0) : (lane < fn ? lane : 0) * 128;
  const unsigned bias = back ? RM : 0;
  auto load_tile = [&](int s) __attribute__((always_inline)) {
    const unsigned src = a_base + (KIND == R_QKV ? s * T40 : item(c, layer, s, 1)), dst = c.lds0 + (s & 1) * BUFB;
#pragma unroll
    for (int i = 0; i < (NFR / 4 + 3) / 4; ++i) {
      const int f = (i * 4 + pw) * 4;
      if (i * 16 + 12 < NFR || f < NFR) dma_sc1n<4>(c.ws, vl, src + (KIND == R_QKV ? f * 1024 : fmap(a_f0 + f)), dst + f * 1024);
    }
  };
  auto arrive = [&](int s) { if (lds_arrive(c.lds0 + CNT + (s & 1) * 4) == (unsigned)(8 * (s >> 1) + 7)) flag_store(c.ws, pgo, s + 1); };
  constexpr int D = 4;
  constexpr bool SWZ_IN = KIND == R_QKV || KIND == R_F1;
  const unsigned vs = KIND == R_WO || KIND == R_F2 ? vl ^ (((o_slice) & 3) * 128) : vl;
  auto fo = [&](int s) -> unsigned { return KIND == R_WO ? fso + (s & 1) * 4096 : fso; };
  volatile LAS unsigned* const mark = (volatile LAS unsigned*)(c.lds + CNT + 32);
  const bool poller = pw == 1;
  const unsigned vp = poller ? vf : 0x80000000u;
  bool pend = false;
  if (first) { __builtin_amdgcn_s_setprio(1); if (KIND != R_QKV || layer) wait_ge(c.ws, vf, fo(0), 1, bias); load_tile(0); }
  BARRIER_V0();
  for (int s = 0; s < NSEG; ++s) {
    STAMP_DECL; STAMP(2);
    const unsigned fn1 = fo(s + 1);
    unsigned pv = 0u;
    ldsp ap[5];
#pragma unroll
    for (int i = 0; i < 5; ++i) ap[i] = c.lds + (s & 1) * BUFB + (KIND == R_WO ? vl + ((5 * wave + i) & 7) * 8192 : (SWZ_IN ? vl ^ ((i & 3) * 128) : vl));
    auto rd = [&](int ks) { return KIND == R_WO ? lds16(ap[ks >> 3] + (ks & 7) * 1024) : lds16(ap[SWZ_IN ? (ks >> 1) & 3 : 0] + ks * 1024); };
    LAS f32x4* const xs = (LAS f32x4*)(c.lds + 2 * BUFB + lane * 16);
    auto dump = [&](int slot, const f32x16& a) {
#pragma unroll
      for (int i = 0; i < 4; ++i) xs[slot * 256 + i * 64] = (f32x4){a[4 * i], a[4 * i + 1], a[4 * i + 2], a[4 * i + 3]};
      FENCE(); mark[slot] = s + 1; };
    f32x16 acc = {};
    f32x4 e0[4], e1[4]; unsigned k0 = 0, k1 = 0;
    const int t0 = wave == 0 ? 0 : (wave == 3 ? 3 : wave - 1), t1 = wave == 2 || wave == 5 ? wave : t0;
    auto peek = [&]() __attribute__((always_inline)) {
      k0 = mark[t0]; k1 = mark[t1];
      FENCE();
#pragma unroll
      for (int i = 0; i < 4; ++i) { e0[i] = xs[t0 * 256 + i * 64]; e1[i] = xs[t1 * 256 + i * 64]; }
      SB(); };
    if (pend) { WAITV(0); arrive(s - 1); STAMP(6); }
    v4u b[D];
#pragma unroll
    for (int i = 0; i < D; ++i) b[i] = rd(i);
    SB();
#pragma unroll
    for (int kb = 0; kb < KS; kb += 8) {
      if (active) {
        if constexpr (KIND == R_WO) {
          if (kb > 0 && ((5 * wave + (kb >> 3)) & 7) == 0) { dump(wave - 1, acc); acc = (f32x16){}; }
        }
#pragma unroll
        for (int ks = kb; ks < kb + 8 && ks < KS; ++ks) {
          if (KIND == R_WO && ks == KS - 4) peek();
          acc = SWP ? mfma32(b[ks % D], W[ks], acc) : mfma32(W[ks], b[ks % D], acc); SB(); if (ks + D < KS) b[ks % D] = rd(ks + D); SB();
        }
      }
      if (kb == 8) { SB(); pv = ld4(c.ws, vp, fn1); SB(); }
    }
    bool got = s + 1 >= NSEG;
    PINV(pv);
    if (poller && !got && all_lanes(pv + bias >= (unsigned)(s + 2))) { FENCE(); mark[7] = s + 1; load_tile(s + 1); got = true; }
    const unsigned ob = o_base + item(c, layer, s, KIND != R_QKV);
    asm volatile("" : "+v"(acc)); STAMP(3);
    bool out = active;
    if constexpr (KIND == R_WO) {
      out = (0xAD >> wave) & 1;
      if (!out) dump(wave, acc);
      else {
        unsigned spins = 0;
        while (UNI(k0) != (unsigned)(s + 1) || UNI(k1) != (unsigned)(s + 1)) { spin_check(c.ws, spins); peek(); }
        const float w1 = t1 != t0 ? 1.f : 0.f;
#pragma unroll
        for (int i = 0; i < 16; ++i) acc[i] += fmaf(e1[i >> 2][i & 3], w1, e0[i >> 2][i & 3]);
      }
    }
    if constexpr (KIND == R_F1) {
#pragma unroll
      for (int i = 0; i < 16; ++i) acc[i] = __builtin_elementwise_maximum(acc[i], 0.f);
    }
    if (out) {
      st16(c.ws, vs, ob, (v4u){pk2(acc[0], acc[1]), pk2(acc[2], acc[3]), pk2(acc[4], acc[5]), pk2(acc[6], acc[7])});
      st16(c.ws, vs, ob + 1024, (v4u){pk2(acc[8], acc[9]), pk2(acc[10], acc[11]), pk2(acc[12], acc[13]), pk2(acc[14], acc[15])});
    }
    FENCE();
    STAMP(5);
    if (first) {
      if (poller) {
        if (!got) {
          pv = ld4(c.ws, vf, fn1);
          asm volatile("s_waitcnt vmcnt(0)" : "+v"(pv) :: "memory");
          got = all_lanes(pv + bias >= (unsigned)(s + 2));
          if (got) { mark[7] = s + 1; load_tile(s + 1); }
          arrive(s);
          if (!got) { wait_ge(c.ws, vf, fn1, s + 2, bias); mark[7] = s + 1; load_tile(s + 1); }
          WAITV(0);
        } else { WAITV(0); arrive(s); }
      } else {
        if (!got && UNI(mark[7]) == (unsigned)(s + 1)) { FENCE(); load_tile(s + 1); got = true; }
        WAITV(0); arrive(s);
        if (!got) { unsigned spins = 0; while (UNI(mark[7]) != (unsigned)(s + 1)) { __builtin_amdgcn_s_sleep(1); spin_check(c.ws, spins); } FENCE(); load_tile(s + 1); STAMP(1); WAITV(0); }
      }
    } else {
      pend = s > 0 && UNI(mark[7]) == (unsigned)(s + 1);
      if (!pend) { WAITV(0); arrive(s); STAMP(4); }
    }
    BARRIER();
    STAMP(0); STAMP_OUT(s + (pw ? 64 : 0), pw == 0 || pw == 4);
  }
}

__device__ __forceinline__ void att_role(const Ctx& c, int idx) {
  const int layer = idx >> 3, h = (idx & 7) * 4 + (c.wave & 3), s0 = c.wave >> 2, lane = c.lane, t = lane & 31, hh = lane >> 5;
  const unsigned vl = lane * 16;
  const unsigned vfq = (lane & 1) ? (unsigned)(32 + (h >> 1)) * 128 : (unsigned)h * 128, fq = c.ring + PA_QKV + layer * BK, fr = c.ring + PA_RH + layer * 4096 + h * 128;
  const unsigned vkm = hh * 256 + (lane & 15) * 16, pgo = c.ringb + PB_ATT + layer * BK + (s0 * 32 + h) * 128;
  const float cs = 0.08838834764831845f * 1.4426950408889634f;
  v4u q[8], k[8], v[8];
  auto ldq = [&](unsigned tb) {
#pragma unroll
    for (int i = 0; i < 8; ++i) q[i] = ld16(c.ws, vl, A_Q + tb + i * 1024); };
  auto ldkv = [&](unsigned tb) {
#pragma unroll
    for (int i = 0; i < 8; ++i) k[i] = ld16(c.ws, vl, A_K + tb + i * 1024);
#pragma unroll
    for (int i = 0; i < 8; ++i) v[i] = ld16(c.ws, vl, A_V + tb + i * 1024); };
  auto pin_all = [&]() {
#pragma unroll
    for (int i = 0; i < 8; ++i) { PINV(q[i]); PINV(k[i]); PINV(v[i]); } };
  wait_ge(c.ws, vfq, fq, s0 + 1);
  ldq(item(c, layer, s0, 0) + h * BK); ldkv(item(c, layer, s0, 0) + h * BK); pin_all();
  for (int s = s0; s < NSEG; s += 2) {
    const unsigned tn = item(c, layer, s + 2, 0) + h * BK, mb = item(c, layer, s, 0) + A_M + h * BK, tb = item(c, layer, s, 1) + B_O + h * BK;
    STAMP_DECL; STAMP(0);
    f32x16 sx = {};
#pragma unroll
    for (int i = 0; i < 8; ++i) sx = mfma32(k[i], q[i], sx);
    float mx = -3.0e38f;
#pragma unroll
    for (int r = 0; r < 16; ++r) { const int key = (r & 3) + 8 * (r >> 2) + 4 * hh; sx[r] = key > t ? -1.0e9f * 1.4426950408889634f : sx[r] * cs; mx = fmaxf(mx, sx[r]); }
    mx = sw32max(mx);
    float lx = 0.f;
#pragma unroll
    for (int r = 0; r < 16; ++r) { sx[r] = __builtin_amdgcn_exp2f(sx[r] - mx); lx += sx[r]; }
    v4u p0 = {pk2(sx[0], sx[1]), pk2(sx[2], sx[3]), pk2(sx[4], sx[5]), pk2(sx[6], sx[7])};
    v4u p1 = {pk2(sx[8], sx[9]), pk2(sx[10], sx[11]), pk2(sx[12], sx[13]), pk2(sx[14], sx[15])};
    f32x16 o[4];
#pragma unroll
    for (int d = 0; d < 4; ++d) { const f32x16 z = {}; o[d] = mfma32(v[2 * d], p0, z); o[d] = mfma32(v[2 * d + 1], p1, o[d]); }
    SB();
    STAMP(2);
    wait_ge(c.ws, 0, fr, s + 1);
    STAMP(3);
    v4u km[8], vm[4];
#pragma unroll
    for (int i = 0; i < 8; ++i) km[i] = ld16(c.ws, vkm, mb + i * 512);
#pragma unroll
    for (int i = 0; i < 4; ++i) vm[i] = ld16(c.ws, vl, mb + 4096 + i * 1024);
    SB();
    f32x16 sm = {};
#pragma unroll
    for (int i = 0; i < 8; ++i) sm = mfma32(km[i], q[i], sm);
    float pm[4], m2 = mx;
#pragma unroll
    for (int r = 0; r < 4; ++r) { pm[r] = (sm[r] + sm[r + 4]) * cs; m2 = fmaxf(m2, pm[r]); }
    m2 = sw32max(m2);
    const float al = __builtin_amdgcn_exp2f(mx - m2);
    float l = lx * al;
#pragma unroll
    for (int r = 0; r < 4; ++r) { pm[r] = __builtin_amdgcn_exp2f(pm[r] - m2); l += pm[r]; }
    l = sw32add(l);
    const float rl = __builtin_amdgcn_rcpf(l), sc = al * rl;
    const unsigned m01 = pk2(pm[0] * rl, pm[1] * rl), m23 = pk2(pm[2] * rl, pm[3] * rl);
    const v4u p2 = {m01, m23, m01, m23};
#pragma unroll
    for (int d = 0; d < 4; ++d) { o[d] *= sc; o[d] = mfma32(vm[d], p2, o[d]); }
#pragma unroll
    for (int d = 0; d < 4; ++d) {
      st16(c.ws, vl, tb + d * 2048, (v4u){pk2(o[d][0], o[d][1]), pk2(o[d][2], o[d][3]), pk2(o[d][4], o[d][5]), pk2(o[d][6], o[d][7])});
      st16(c.ws, vl, tb + d * 2048 + 1024, (v4u){pk2(o[d][8], o[d][9]), pk2(o[d][10], o[d][11]), pk2(o[d][12], o[d][13]), pk2(o[d][14], o[d][15])});
    }
    FENCE();
    STAMP(5);
    unsigned pv = ld4(c.ws, vfq, fq);
    asm volatile("s_waitcnt vmcnt(0)" : "+v"(pv) :: "memory");
    flag_store(c.ws, pgo, s + 1);
    STAMP(6);
    if (s + 2 < NSEG) {
      if (!all_lanes(pv >= (unsigned)(s + 3))) wait_ge(c.ws, vfq, fq, s + 3);
#pragma unroll
      for (int i = 0; i < 24; ++i) {
        v4u& d = i < 8 ? q[i] : (i < 16 ? k[i - 8] : v[i - 16]);
        d = ld16(c.ws, vl, (i < 8 ? A_Q : (i < 16 ? A_K : A_V)) + tn + (i & 7) * 1024);
        SB(); if (i >= PACE) waitv<PACE>();
      }
    }
    SB();
    STAMP(4); STAMP_OUT(s, (c.wave & 3) == 0);
  }
}

template <int NP, bool FINAL>
__device__ __forceinline__ void red_loop(const Ctx& c, int idx) {
  const int stream = idx >> 2, tg = idx & 3, layer = stream >> 1, wave = c.wave & 3, lane = c.lane, g = lane >> 4, n = lane & 15;
  const unsigned lb = (c.wave >> 2) * 1024;
  const int fn = NP == 4 ? 16 : 9;
  const unsigned fso = c.ringb + (NP == 4 ? PB_WO + layer * 2048 : PB_F2 + layer * 9 * 128), pgo = c.ringb + PB_RED + idx * 128;
  const unsigned vf = (NP == 4 && layer && lane == 63) ? c.ringb + PB_RED + (4 + tg) * 128 - fso : (lane < fn ? lane : 0) * 128;
  const rsrc_t rr = layer ? c.ws : mkrsrc(c.p.x, 2048 * 1280);
  const unsigned xo = NP == 4 ? B_XB : XA_O, no = NP == 4 ? B_XN2 : XN1_O + 64 * T40;
  const unsigned vt = (n >> 3) * 512 + (tg * 8 + (n & 7)) * 16;
  auto sw = [&](int t) -> unsigned { return ((5 * wave + t) & 3) * 128; };
  auto om = [&](int f) -> unsigned { return NP == 4 ? fmap(f) : f * 1024; };
  const unsigned vi = vt + (g < NP ? (NP == 4 ? B_P1 : B_P2) + g * 5 * BK : B_XB);
  const unsigned vo = vt + (wave * 10 + (g >> 1)) * 1024 + (g & 1) * 8;
  const unsigned vx = (tg * 8 + (n & 7)) * 1280 + (16 * (wave * 10 + (g >> 1)) + 8 * (g & 1) + 4 * (n >> 3)) * 2;
  const unsigned vp0 = (vt ^ sw(g & 1)) + om(wave * 10 + (g >> 1) + (g & 1) * 2), vp1 = (vt ^ sw(2 + (g & 1))) + om(wave * 10 + 4 + (g >> 1) + (g & 1) * 2);
  const unsigned vq = (vt ^ sw(4)) + om(wave * 10 + 8 + (g >> 1)) + (g & 1 ? no : xo);
  v4u sel[2];
#pragma unroll
  for (int i = 0; i < 4; ++i) { const unsigned e = (n & 7) >> 1 == i ? (n & 1 ? 0x3F800000u : 0x3F80u) : 0u; sel[0][i] = n < 8 ? e : 0u; sel[1][i] = n < 8 ? 0u : e; }
  volatile LAS unsigned* const kn = (volatile LAS unsigned*)(c.lds + lb + 224);
  const unsigned vw = wave == 0 ? vf : 0x80000000u;
  unsigned pv = 0; bool have = false, got_r = false; v4u r4[2]; v2u r2; v4u u[10];
  const unsigned vr0 = layer ? (vt ^ sw(g & 1)) + (wave * 10 + (g >> 1) + (g & 1) * 2) * 1024 : (tg * 8 + (n & 7)) * 1280 + (160 * wave + 8 * g + 32 * (n >> 3)) * 2;
  const unsigned vr1 = layer ? (vr0 ^ 256) + 4096 : vr0 + 128, vr2 = layer ? (vo ^ sw(4)) + 8192 : vx + 256;
  auto ldr = [&](int s) {
    const unsigned o = s * T40 + (layer ? XA_O : 0);
    r4[0] = ld16(rr, vr0, o); r4[1] = ld16(rr, vr1, o); r2 = __builtin_amdgcn_raw_buffer_load_b64(rr, vr2, o, 16); };
  auto ldu = [&](int s) {
    const unsigned ib = item(c, layer, s, 1);
#pragma unroll
    for (int i = 0; i < 10; ++i) u[i] = ld16(c.ws, vi ^ sw(i >> 1), ib + fmap(wave * 10 + i)); };
  auto tell = [&](int n) {
    PINV(pv);
    if (wave == 0) { const unsigned long long m = __builtin_amdgcn_ballot_w64(pv >= (unsigned)n); FENCE();
      if (m == ~0ull) kn[0] = n; if (NP == 4 && (m >> 63)) kn[1] = n; } };
  for (int s = 0; s < NSEG; ++s) {
    STAMP_DECL; STAMP(0);
    f32x4 a[5];
    const unsigned so = NP == 4 ? item(c, layer, s, 1) : s * T40;
    if (!have) {
      if (wave == 0) { if (!all_lanes(pv > (unsigned)s)) wait_ge(c.ws, vf, fso, s + 1); kn[0] = s + 1; }
      else { unsigned spins = 0; while (UNI(kn[0]) <= (unsigned)s) { __builtin_amdgcn_s_sleep(1); spin_check(c.ws, spins); } FENCE(); }
      STAMP(5);
      if (NP == 4 && !got_r) ldr(s);
      ldu(s);
      FENCE(); st4(c.ws, 0x80000000u, 0, 0u);
    }
    SB();
    STAMP(1);
    if constexpr (NP == 4) {
      auto un = [](unsigned x, unsigned y) { return (f32x4){blo(x), bhi(x), blo(y), bhi(y)}; };
#pragma unroll
      for (int j = 0; j < 2; ++j) {
        const v4u q = r4[j];
        if (layer) {
          auto x = __builtin_amdgcn_permlane16_swap(q.x, q.z, false, false), y = __builtin_amdgcn_permlane16_swap(q.y, q.w, false, false);
          a[2 * j] = un(x[0], y[0]); a[2 * j + 1] = un(x[1], y[1]);
        } else {
          a[2 * j] = un(__builtin_amdgcn_update_dpp(q.x, q.z, 0x128, 0xf, 0xc, false), __builtin_amdgcn_update_dpp(q.y, q.w, 0x128, 0xf, 0xc, false));
          a[2 * j + 1] = un(__builtin_amdgcn_update_dpp(q.z, q.x, 0x128, 0xf, 0x3, false), __builtin_amdgcn_update_dpp(q.w, q.y, 0x128, 0xf, 0x3, false));
        }
      }
      a[4] = un(r2.x, r2.y);
    } else {
#pragma unroll
      for (int t = 0; t < 5; ++t) a[t] = (f32x4){0.f, 0.f, 0.f, 0.f};
    }
#pragma unroll
    for (int i = 0; i < 10; ++i) { if (i == 2) { SB(); pv = ld4(c.ws, vw, fso); SB(); } a[i >> 1] = mfma16(sel[i & 1], u[i], a[i >> 1]); }
    STAMP(2);
    if constexpr (FINAL) {
      auto pair8 = [&](const f32x4& p, const f32x4& q) {
        const unsigned p0 = pk2(p[0], p[1]), p1 = pk2(p[2], p[3]), q0 = pk2(q[0], q[1]), q1 = pk2(q[2], q[3]);
        return (v4u){(unsigned)__builtin_amdgcn_update_dpp(p0, q0, 0x128, 0xf, 0xc, false), (unsigned)__builtin_amdgcn_update_dpp(p1, q1, 0x128, 0xf, 0xc, false),
                     (unsigned)__builtin_amdgcn_update_dpp(q0, p0, 0x128, 0xf, 0x3, false), (unsigned)__builtin_amdgcn_update_dpp(q1, p1, 0x128, 0xf, 0x3, false)}; };
      unsigned short* o = c.p.out + (size_t)(s * 32 + tg * 8 + (n & 7)) * DM + 160 * wave + 8 * g;
      *(v4u*)(o + 32 * (n >> 3)) = pair8(a[0], a[1]); *(v4u*)(o + 64 + 32 * (n >> 3)) = pair8(a[2], a[3]);
      *(v2u*)(o + 128 + 4 * (n >> 3)) = (v2u){pk2(a[4][0], a[4][1]), pk2(a[4][2], a[4][3])};
      FENCE();
      tell(s + 2);
      have = UNI(kn[0]) > (unsigned)(s + 1);
      if (have) ldu(s + 1);
      *(volatile LAS unsigned*)(c.lds + lb + 192 + wave * 4) = s + 1;
      { const v4u d = *(volatile LAS v4u*)(c.lds + lb + 192); st4(c.ws, lane == 0 && wave ? 0u : 0x80000000u, pgo, umin(umin(d.x, d.y), umin(d.z, d.w))); }
    } else {
      f32x2 s1 = {0.f, 0.f}, s2 = {0.f, 0.f};
#pragma unroll
      for (int t = 0; t < 5; ++t) { const f32x2 lo = {a[t][0], a[t][1]}, hi = {a[t][2], a[t][3]}; s1 += lo; s1 += hi; s2 += lo * lo; s2 += hi * hi; }
      float m1 = s1[0] + s1[1], m2 = s2[0] + s2[1];
      m1 += ror8(m1); m2 += ror8(m2);
      { auto x = __builtin_amdgcn_permlane16_swap(__float_as_uint(m1), __float_as_uint(m2), false, false); m1 = sw32add(__uint_as_float(x[0]) + __uint_as_float(x[1])); }
      const ldsp red = c.lds + lb + 256 + (s & 1) * 256;
      if (lane < 32 && n < 8) *(LAS float*)(red + n * 32 + g * 16 + wave * 4) = m1;
      const unsigned got = lds_arrive(c.lds0 + lb + 64);
      auto pair16 = [](const f32x4& p, const f32x4& q) {
        auto r0 = __builtin_amdgcn_permlane16_swap(pk2(p[0], p[1]), pk2(q[0], q[1]), false, false);
        auto r1 = __builtin_amdgcn_permlane16_swap(pk2(p[2], p[3]), pk2(q[2], q[3]), false, false);
        return (v4u){r0[0], r1[0], r0[1], r1[1]}; };
      if (got < (unsigned)(4 * s + 3)) {
        unsigned spins = 0;
        while (UNI(*(volatile LAS unsigned*)(c.lds + lb + 64)) < (unsigned)(4 * s + 4)) spin_check(c.ws, spins);
      }
      FENCE();
      const f32x4 e0 = *(LAS f32x4*)(red + (lane & 7) * 32), e1 = *(LAS f32x4*)(red + (lane & 7) * 32 + 16);
      tell(s + 2);
      const float mean = ((e0[0] + e0[1]) + (e0[2] + e0[3])) * (1.0f / 640.0f);
      const float var = fmaxf(((e1[0] + e1[1]) + (e1[2] + e1[3])) * (1.0f / 640.0f) - mean * mean, 0.f);
      const float rs = __builtin_amdgcn_rsqf(var + 1e-5f), nb = -mean * rs;
      st16(c.ws, vp0, no + so, pair16(a[0] * rs + nb, a[1] * rs + nb)); st16(c.ws, vp1, no + so, pair16(a[2] * rs + nb, a[3] * rs + nb));
      st16(c.ws, vq, so, pair16(a[4], a[4] * rs + nb));
      st16(c.ws, vp0, xo + so, pair16(a[0], a[1])); st16(c.ws, vp1, xo + so, pair16(a[2], a[3]));
      STAMP(3);
      FENCE();
      have = UNI(kn[0]) > (unsigned)(s + 1); got_r = NP == 4 && s + 1 < NSEG && (layer == 0 || UNI(kn[1]) > (unsigned)(s + 1));
      FENCE();
      if (have) { if (NP == 4) ldr(s + 1); ldu(s + 1); FENCE(); waitv<NP == 4 ? 13 : 10>(); }
      else { pv = ld4(c.ws, vw, fso); FENCE(); if (got_r) { ldr(s + 1); FENCE(); WAITV(4); } else WAITV(1); }
      st4(c.ws, lds_arrive(c.lds0 + lb + 128) == (unsigned)(4 * s + 3) && lane == 0 ? 0u : 0x80000000u, pgo, s + 1);
    }
    STAMP(4); STAMP_OUT((c.wave >> 2) * 64 + s, wave == 0);
  }
}
__device__ __forceinline__ void red_role(const Ctx& c, int cu) {
  const int idx = c.wave < 4 ? cu : (REBAL ? cu ^ 12 : cu + 8), stream = idx >> 2;
  if (c.wave < 4 && REBAL > 1) __builtin_amdgcn_s_setprio(1);
  if (stream == 3) red_loop<3, true>(c, idx); else if (stream == 1) red_loop<3, false>(c, idx); else red_loop<4, false>(c, idx);
}

constexpr unsigned RH_PM = 18944, RH_SL = 32768;
constexpr unsigned OOB = 0x80000000u;
#define RH_ASM_N \
  "v_xor_b32 v242, 64, v240\n" \
  "v_add_u32 v242, 31744, v242\n" \
  "v_add_u32 v243, 0x10000, v241\n" \
  "s_add_u32 m0, s78, 32768\n" \
  "s_add_u32 s74, s79, 0\n" \
  "buffer_load_dwordx4 v240, s[44:47], s74 offen lds\n" \
  "buffer_load_dwordx4 v242, s[44:47], s74 offen offset:1024 lds\n" \
  "s_add_u32 m0, s78, 49152\n" \
  "s_add_u32 s74, s79, 524288\n" \
  "buffer_load_dwordx4 v240, s[44:47], s74 offen lds\n" \
  "buffer_load_dwordx4 v242, s[44:47], s74 offen offset:1024 lds\n" \
  "s_add_u32 m0, s78, 65536\n" \
  "s_add_u32 s74, s79, 1048576\n" \
  "buffer_load_dwordx4 v240, s[44:47], s74 offen lds\n" \
  "buffer_load_dwordx4 v242, s[44:47], s74 offen offset:1024 lds\n" \
  "s_add_u32 m0, s78, 81920\n" \
  "s_add_u32 s74, s79, 1572864\n" \
  "buffer_load_dwordx4 v240, s[44:47], s74 offen lds\n" \
  "buffer_load_dwordx4 v242, s[44:47], s74 offen offset:1024 lds\n" \
  "s_add_u32 m0, s78, 98304\n" \
  "s_add_u32 s74, s79, 2097152\n" \
  "buffer_load_dwordx4 v240, s[44:47], s74 offen lds\n" \
  "buffer_load_dwordx4 v242, s[44:47], s74 offen offset:1024 lds\n" \
  "s_add_u32 m0, s78, 114688\n" \
  "s_add_u32 s74, s79, 2621440\n" \
  "buffer_load_dwordx4 v240, s[44:47], s74 offen lds\n" \
  "buffer_load_dwordx4 v242, s[44:47], s74 offen offset:1024 lds\n" \
  "s_add_u32 m0, s78, 0\n" \
  "s_add_u32 s74, s79, 3145728\n" \
  "buffer_load_dwordx4 v240, s[44:47], s74 offen lds\n" \
  "buffer_load_dwordx4 v242, s[44:47], s74 offen offset:1024 lds\n" \
  "s_add_u32 m0, s78, 16384\n" \
  "s_add_u32 s74, s79, 3670016\n" \
  "buffer_load_dwordx4 v240, s[44:47], s74 offen lds\n" \
  "buffer_load_dwordx4 v242, s[44:47], s74 offen offset:1024 lds\n" \
  "s_waitcnt vmcnt(14) lgkmcnt(0)\n" \
  "s_barrier\n" \
  "ds_read_b64_tr_b16 v[0:1], v241 offset:32768\n" \
  "ds_read_b64_tr_b16 v[2:3], v241 offset:33280\n" \
  "ds_read_b64_tr_b16 v[4:5], v241 offset:40960\n" \
  "ds_read_b64_tr_b16 v[6:7], v241 offset:41472\n" \
  "s_waitcnt lgkmcnt(0)\n" \
  "s_barrier\n" \
  "s_add_u32 m0, s78, 32768\n" \
  "s_add_u32 s74, s79, 4194304\n" \
  "buffer_load_dwordx4 v240, s[44:47], s74 offen lds\n" \
  "buffer_load_dwordx4 v242, s[44:47], s74 offen offset:1024 lds\n" \
  "s_waitcnt vmcnt(14) lgkmcnt(0)\n" \
  "s_barrier\n" \
  "ds_read_b64_tr_b16 v[8:9], v241 offset:49152\n" \
  "ds_read_b64_tr_b16 v[10:11], v241 offset:49664\n" \
  "ds_read_b64_tr_b16 v[12:13], v241 offset:57344\n" \
  "ds_read_b64_tr_b16 v[14:15], v241 offset:57856\n" \
  "s_waitcnt lgkmcnt(0)\n" \
  "s_barrier\n" \
  "s_add_u32 m0, s78, 49152\n" \
  "s_add_u32 s74, s79, 0\n" \
  "buffer_load_dwordx4 v240, s[48:51], s74 offen lds\n" \
  "buffer_load_dwordx4 v242, s[48:51], s74 offen offset:1024 lds\n" \
  "s_waitcnt vmcnt(14) lgkmcnt(0)\n" \
  "s_barrier\n" \
  "ds_read_b64_tr_b16 v[16:17], v243 offset:0\n" \
  "ds_read_b64_tr_b16 v[18:19], v243 offset:512\n" \
  "ds_read_b64_tr_b16 v[20:21], v243 offset:8192\n" \
  "ds_read_b64_tr_b16 v[22:23], v243 offset:8704\n" \
  "s_waitcnt lgkmcnt(0)\n" \
  "s_barrier\n" \
  "s_add_u32 m0, s78, 65536\n" \
  "s_add_u32 s74, s79, 524288\n" \
  "buffer_load_dwordx4 v240, s[48:51], s74 offen lds\n" \
  "buffer_load_dwordx4 v242, s[48:51], s74 offen offset:1024 lds\n" \
  "s_waitcnt vmcnt(14) lgkmcnt(0)\n" \
  "s_barrier\n" \
  "ds_read_b64_tr_b16 v[24:25], v243 offset:16384\n" \
  "ds_read_b64_tr_b16 v[26:27], v243 offset:16896\n" \
  "ds_read_b64_tr_b16 v[28:29], v243 offset:24576\n" \
  "ds_read_b64_tr_b16 v[30:31], v243 offset:25088\n" \
  "s_waitcnt lgkmcnt(0)\n" \
  "s_barrier\n" \
  "s_add_u32 m0, s78, 81920\n" \
  "s_add_u32 s74, s79, 1048576\n" \
  "buffer_load_dwordx4 v240, s[48:51], s74 offen lds\n" \
  "buffer_load_dwordx4 v242, s[48:51], s74 offen offset:1024 lds\n" \
  "s_waitcnt vmcnt(14) lgkmcnt(0)\n" \
  "s_barrier\n" \
  "ds_read_b64_tr_b16 v[32:33], v243 offset:32768\n" \
  "ds_read_b64_tr_b16 v[34:35], v243 offset:33280\n" \
  "ds_read_b64_tr_b16 v[36:37], v243 offset:40960\n" \
  "ds_read_b64_tr_b16 v[38:39], v243 offset:41472\n" \
  "s_waitcnt lgkmcnt(0)\n" \
  "s_barrier\n" \
  "s_add_u32 m0, s78, 98304\n" \
  "s_add_u32 s74, s79, 1572864\n" \
  "buffer_load_dwordx4 v240, s[48:51], s74 offen lds\n" \
  "buffer_load_dwordx4 v242, s[48:51], s74 offen offset:1024 lds\n" \
  "s_waitcnt vmcnt(14) lgkmcnt(0)\n" \
  "s_barrier\n" \
  "ds_read_b64_tr_b16 v[40:41], v243 offset:49152\n" \
  "ds_read_b64_tr_b16 v[42:43], v243 offset:49664\n" \
  "ds_read_b64_tr_b16 v[44:45], v243 offset:57344\n" \
  "ds_read_b64_tr_b16 v[46:47], v243 offset:57856\n" \
  "s_waitcnt lgkmcnt(0)\n" \
  "s_barrier\n" \
  "s_add_u32 m0, s78, 114688\n" \
  "s_add_u32 s74, s79, 2097152\n" \
  "buffer_load_dwordx4 v240, s[48:51], s74 offen lds\n" \
  "buffer_load_dwordx4 v242, s[48:51], s74 offen offset:1024 lds\n" \
  "s_waitcnt vmcnt(14) lgkmcnt(0)\n" \
  "s_barrier\n" \
  "ds_read_b64_tr_b16 v[48:49], v241 offset:0\n" \
  "ds_read_b64_tr_b16 v[50:51], v241 offset:512\n" \
  "ds_read_b64_tr_b16 v[52:53], v241 offset:8192\n" \
  "ds_read_b64_tr_b16 v[54:55], v241 offset:8704\n" \
  "s_waitcnt lgkmcnt(0)\n" \
  "s_barrier\n" \
  "s_add_u32 m0, s78, 0\n" \
  "s_add_u32 s74, s79, 2621440\n" \
  "buffer_load_dwordx4 v240, s[48:51], s74 offen lds\n" \
  "buffer_load_dwordx4 v242, s[48:51], s74 offen offset:1024 lds\n" \
  "s_waitcnt vmcnt(14) lgkmcnt(0)\n" \
  "s_barrier\n" \
  "ds_read_b64_tr_b16 v[56:57], v241 offset:16384\n" \
  "ds_read_b64_tr_b16 v[58:59], v241 offset:16896\n" \
  "ds_read_b64_tr_b16 v[60:61], v241 offset:24576\n" \
  "ds_read_b64_tr_b16 v[62:63], v241 offset:25088\n" \
  "s_waitcnt lgkmcnt(0)\n" \
  "s_barrier\n" \
  "s_add_u32 m0, s78, 16384\n" \
  "s_add_u32 s74, s79, 3145728\n" \
  "buffer_load_dwordx4 v240, s[48:51], s74 offen lds\n" \
  "buffer_load_dwordx4 v242, s[48:51], s74 offen offset:1024 lds\n" \
  "s_waitcnt vmcnt(14) lgkmcnt(0)\n" \
  "s_barrier\n" \
  "ds_read_b64_tr_b16 v[64:65], v241 offset:32768\n" \
  "ds_read_b64_tr_b16 v[66:67], v241 offset:33280\n" \
  "ds_read_b64_tr_b16 v[68:69], v241 offset:40960\n" \
  "ds_read_b64_tr_b16 v[70:71], v241 offset:41472\n" \
  "s_waitcnt lgkmcnt(0)\n" \
  "s_barrier\n" \
  "s_add_u32 m0, s78, 32768\n" \
  "s_add_u32 s74, s79, 3670016\n" \
  "buffer_load_dwordx4 v240, s[48:51], s74 offen lds\n" \
  "buffer_load_dwordx4 v242, s[48:51], s74 offen offset:1024 lds\n" \
  "s_waitcnt vmcnt(14) lgkmcnt(0)\n" \
  "s_barrier\n" \
  "ds_read_b64_tr_b16 v[72:73], v241 offset:49152\n" \
  "ds_read_b64_tr_b16 v[74:75], v241 offset:49664\n" \
  "ds_read_b64_tr_b16 v[76:77], v241 offset:57344\n" \
  "ds_read_b64_tr_b16 v[78:79], v241 offset:57856\n" \
  "s_waitcnt lgkmcnt(0)\n" \
  "s_barrier\n" \
  "s_add_u32 m0, s78, 49152\n" \
  "s_add_u32 s74, s79, 4194304\n" \
  "buffer_load_dwordx4 v240, s[48:51], s74 offen lds\n" \
  "buffer_load_dwordx4 v242, s[48:51], s74 offen offset:1024 lds\n" \
  "s_waitcnt vmcnt(14) lgkmcnt(0)\n" \
  "s_barrier\n" \
  "ds_read_b64_tr_b16 v[80:81], v243 offset:0\n" \
  "ds_read_b64_tr_b16 v[82:83], v243 offset:512\n" \
  "ds_read_b64_tr_b16 v[84:85], v243 offset:8192\n" \
  "ds_read_b64_tr_b16 v[86:87], v243 offset:8704\n" \
  "s_waitcnt lgkmcnt(0)\n" \
  "s_barrier\n" \
  "s_add_u32 m0, s78, 65536\n" \
  "s_add_u32 s74, s79, 0\n" \
  "buffer_load_dwordx4 v240, s[52:55], s74 offen lds\n" \
  "buffer_load_dwordx4 v242, s[52:55], s74 offen offset:1024 lds\n" \
  "s_waitcnt vmcnt(14) lgkmcnt(0)\n" \
  "s_barrier\n" \
  "ds_read_b64_tr_b16 v[88:89], v243 offset:16384\n" \
  "ds_read_b64_tr_b16 v[90:91], v243 offset:16896\n" \
  "ds_read_b64_tr_b16 v[92:93], v243 offset:24576\n" \
  "ds_read_b64_tr_b16 v[94:95], v243 offset:25088\n" \
  "s_waitcnt lgkmcnt(0)\n" \
  "s_barrier\n" \
  "s_add_u32 m0, s78, 81920\n" \
  "s_add_u32 s74, s79, 524288\n" \
  "buffer_load_dwordx4 v240, s[52:55], s74 offen lds\n" \
  "buffer_load_dwordx4 v242, s[52:55], s74 offen offset:1024 lds\n" \
  "s_waitcnt vmcnt(14) lgkmcnt(0)\n" \
  "s_barrier\n" \
  "ds_read_b64_tr_b16 v[96:97], v243 offset:32768\n" \
  "ds_read_b64_tr_b16 v[98:99], v243 offset:33280\n" \
  "ds_read_b64_tr_b16 v[100:101], v243 offset:40960\n" \
  "ds_read_b64_tr_b16 v[102:103], v243 offset:41472\n" \
  "s_waitcnt lgkmcnt(0)\n" \
  "s_barrier\n" \
  "s_add_u32 m0, s78, 98304\n" \
  "s_add_u32 s74, s79, 1048576\n" \
  "buffer_load_dwordx4 v240, s[52:55], s74 offen lds\n" \
  "buffer_load_dwordx4 v242, s[52:55], s74 offen offset:1024 lds\n" \
  "s_waitcnt vmcnt(14) lgkmcnt(0)\n" \
  "s_barrier\n" \
  "ds_read_b64_tr_b16 v[104:105], v243 offset:49152\n" \
  "ds_read_b64_tr_b16 v[106:107], v243 offset:49664\n" \
  "ds_read_b64_tr_b16 v[108:109], v243 offset:57344\n" \
  "ds_read_b64_tr_b16 v[110:111], v243 offset:57856\n" \
  "s_waitcnt lgkmcnt(0)\n" \
  "s_barrier\n" \
  "s_add_u32 m0, s78, 114688\n" \
  "s_add_u32 s74, s79, 1572864\n" \
  "buffer_load_dwordx4 v240, s[52:55], s74 offen lds\n" \
  "buffer_load_dwordx4 v242, s[52:55], s74 offen offset:1024 lds\n" \
  "s_waitcnt vmcnt(14) lgkmcnt(0)\n" \
  "s_barrier\n" \
  "ds_read_b64_tr_b16 v[112:113], v241 offset:0\n" \
  "ds_read_b64_tr_b16 v[114:115], v241 offset:512\n" \
  "ds_read_b64_tr_b16 v[116:117], v241 offset:8192\n" \
  "ds_read_b64_tr_b16 v[118:119], v241 offset:8704\n" \
  "s_waitcnt lgkmcnt(0)\n" \
  "s_barrier\n" \
  "s_add_u32 m0, s78, 0\n" \
  "s_add_u32 s74, s79, 4718592\n" \
  "buffer_load_dwordx4 v240, s[44:47], s74 offen lds\n" \
  "buffer_load_dwordx4 v242, s[44:47], s74 offen offset:1024 lds\n" \
  "s_waitcnt vmcnt(14) lgkmcnt(0)\n" \
  "s_barrier\n" \
  "ds_read_b64_tr_b16 v[120:121], v241 offset:16384\n" \
  "ds_read_b64_tr_b16 v[122:123], v241 offset:16896\n" \
  "ds_read_b64_tr_b16 v[124:125], v241 offset:24576\n" \
  "ds_read_b64_tr_b16 v[126:127], v241 offset:25088\n" \
  "s_waitcnt lgkmcnt(0)\n" \
  "s_barrier\n" \
  "s_add_u32 m0, s78, 16384\n" \
  "s_add_u32 s74, s79, 4718592\n" \
  "buffer_load_dwordx4 v240, s[48:51], s74 offen lds\n" \
  "buffer_load_dwordx4 v242, s[48:51], s74 offen offset:1024 lds\n" \
  "s_waitcnt vmcnt(14) lgkmcnt(0)\n" \
  "s_barrier\n" \
  "ds_read_b64_tr_b16 v[128:129], v241 offset:32768\n" \
  "ds_read_b64_tr_b16 v[130:131], v241 offset:33280\n" \
  "ds_read_b64_tr_b16 v[132:133], v241 offset:40960\n" \
  "ds_read_b64_tr_b16 v[134:135], v241 offset:41472\n" \
  "s_waitcnt lgkmcnt(0)\n" \
  "s_barrier\n" \
  "s_add_u32 m0, s78, 32768\n" \
  "s_add_u32 s74, s79, 2097152\n" \
  "buffer_load_dwordx4 v240, s[52:55], s74 offen lds\n" \
  "buffer_load_dwordx4 v242, s[52:55], s74 offen offset:1024 lds\n" \
  "s_waitcnt vmcnt(14) lgkmcnt(0)\n" \
  "s_barrier\n" \
  "ds_read_b64_tr_b16 v[136:137], v241 offset:49152\n" \
  "ds_read_b64_tr_b16 v[138:139], v241 offset:49664\n" \
  "ds_read_b64_tr_b16 v[140:141], v241 offset:57344\n" \
  "ds_read_b64_tr_b16 v[142:143], v241 offset:57856\n" \
  "s_waitcnt lgkmcnt(0)\n" \
  "s_barrier\n" \
  "s_add_u32 m0, s78, 49152\n" \
  "s_add_u32 s74, s79, 2621440\n" \
  "buffer_load_dwordx4 v240, s[52:55], s74 offen lds\n" \
  "buffer_load_dwordx4 v242, s[52:55], s74 offen offset:1024 lds\n" \
  "s_waitcnt vmcnt(14) lgkmcnt(0)\n" \
  "s_barrier\n" \
  "ds_read_b64_tr_b16 v[144:145], v243 offset:0\n" \
  "ds_read_b64_tr_b16 v[146:147], v243 offset:512\n" \
  "ds_read_b64_tr_b16 v[148:149], v243 offset:8192\n" \
  "ds_read_b64_tr_b16 v[150:151], v243 offset:8704\n" \
  "s_waitcnt lgkmcnt(0)\n" \
  "s_barrier\n" \
  "s_add_u32 m0, s78, 65536\n" \
  "s_add_u32 s74, s79, 3145728\n" \
  "buffer_load_dwordx4 v240, s[52:55], s74 offen lds\n" \
  "buffer_load_dwordx4 v242, s[52:55], s74 offen offset:1024 lds\n" \
  "s_waitcnt vmcnt(14) lgkmcnt(0)\n" \
  "s_barrier\n" \
  "ds_read_b64_tr_b16 v[152:153], v243 offset:16384\n" \
  "ds_read_b64_tr_b16 v[154:155], v243 offset:16896\n" \
  "ds_read_b64_tr_b16 v[156:157], v243 offset:24576\n" \
  "ds_read_b64_tr_b16 v[158:159], v243 offset:25088\n" \
  "s_waitcnt lgkmcnt(0)\n" \
  "s_barrier\n" \
  "s_add_u32 m0, s78, 81920\n" \
  "s_add_u32 s74, s79, 3670016\n" \
  "buffer_load_dwordx4 v240, s[52:55], s74 offen lds\n" \
  "buffer_load_dwordx4 v242, s[52:55], s74 offen offset:1024 lds\n" \
  "s_waitcnt vmcnt(14) lgkmcnt(0)\n" \
  "s_barrier\n" \
  "ds_read_b64_tr_b16 v[160:161], v243 offset:32768\n" \
  "ds_read_b64_tr_b16 v[162:163], v243 offset:33280\n" \
  "ds_read_b64_tr_b16 v[164:165], v243 offset:40960\n" \
  "ds_read_b64_tr_b16 v[166:167], v243 offset:41472\n" \
  "s_waitcnt lgkmcnt(0)\n" \
  "s_barrier\n" \
  "s_add_u32 m0, s78, 98304\n" \
  "s_add_u32 s74, s79, 4194304\n" \
  "buffer_load_dwordx4 v240, s[52:55], s74 offen lds\n" \
  "buffer_load_dwordx4 v242, s[52:55], s74 offen offset:1024 lds\n" \
  "s_waitcnt vmcnt(14) lgkmcnt(0)\n" \
  "s_barrier\n" \
  "ds_read_b64_tr_b16 v[168:169], v243 offset:49152\n" \
  "ds_read_b64_tr_b16 v[170:171], v243 offset:49664\n" \
  "ds_read_b64_tr_b16 v[172:173], v243 offset:57344\n" \
  "ds_read_b64_tr_b16 v[174:175], v243 offset:57856\n" \
  "s_waitcnt lgkmcnt(0)\n" \
  "s_barrier\n" \
  "s_add_u32 m0, s78, 114688\n" \
  "s_add_u32 s74, s79, 4718592\n" \
  "buffer_load_dwordx4 v240, s[52:55], s74 offen lds\n" \
  "buffer_load_dwordx4 v242, s[52:55], s74 offen offset:1024 lds\n" \
  "s_waitcnt vmcnt(14) lgkmcnt(0)\n" \
  "s_barrier\n" \
  "ds_read_b64_tr_b16 v[224:225], v241 offset:0\n" \
  "ds_read_b64_tr_b16 v[226:227], v241 offset:512\n" \
  "ds_read_b64_tr_b16 v[228:229], v241 offset:8192\n" \
  "ds_read_b64_tr_b16 v[230:231], v241 offset:8704\n" \
  "s_waitcnt lgkmcnt(0)\n" \
  "s_barrier\n" \
  "ds_write_b128 v245, v[224:227] offset:0\n" \
  "ds_write_b128 v245, v[228:231] offset:1024\n" \
  "s_waitcnt vmcnt(12) lgkmcnt(0)\n" \
  "s_barrier\n" \
  "ds_read_b64_tr_b16 v[224:225], v241 offset:16384\n" \
  "ds_read_b64_tr_b16 v[226:227], v241 offset:16896\n" \
  "ds_read_b64_tr_b16 v[228:229], v241 offset:24576\n" \
  "ds_read_b64_tr_b16 v[230:231], v241 offset:25088\n" \
  "s_waitcnt lgkmcnt(0)\n" \
  "s_barrier\n" \
  "ds_write_b128 v245, v[224:227] offset:16384\n" \
  "ds_write_b128 v245, v[228:231] offset:17408\n" \
  "s_waitcnt vmcnt(10) lgkmcnt(0)\n" \
  "s_barrier\n" \
  "ds_read_b64_tr_b16 v[224:225], v241 offset:32768\n" \
  "ds_read_b64_tr_b16 v[226:227], v241 offset:33280\n" \
  "ds_read_b64_tr_b16 v[228:229], v241 offset:40960\n" \
  "ds_read_b64_tr_b16 v[230:231], v241 offset:41472\n" \
  "s_waitcnt lgkmcnt(0)\n" \
  "s_barrier\n" \
  "ds_write_b128 v245, v[224:227] offset:32768\n" \
  "ds_write_b128 v245, v[228:231] offset:33792\n" \
  "s_waitcnt vmcnt(8) lgkmcnt(0)\n" \
  "s_barrier\n" \
  "ds_read_b64_tr_b16 v[224:225], v241 offset:49152\n" \
  "ds_read_b64_tr_b16 v[226:227], v241 offset:49664\n" \
  "ds_read_b64_tr_b16 v[228:229], v241 offset:57344\n" \
  "ds_read_b64_tr_b16 v[230:231], v241 offset:57856\n" \
  "s_waitcnt lgkmcnt(0)\n" \
  "s_barrier\n" \
  "ds_write_b128 v245, v[224:227] offset:49152\n" \
  "ds_write_b128 v245, v[228:231] offset:50176\n" \
  "s_waitcnt vmcnt(6) lgkmcnt(0)\n" \
  "s_barrier\n" \
  "ds_read_b64_tr_b16 v[224:225], v243 offset:0\n" \
  "ds_read_b64_tr_b16 v[226:227], v243 offset:512\n" \
  "ds_read_b64_tr_b16 v[228:229], v243 offset:8192\n" \
  "ds_read_b64_tr_b16 v[230:231], v243 offset:8704\n" \
  "s_waitcnt lgkmcnt(0)\n" \
  "s_barrier\n" \
  "v_add_u32 v242, 0x10000, v245\n" \
  "ds_write_b128 v242, v[224:227] offset:0\n" \
  "ds_write_b128 v242, v[228:231] offset:1024\n" \
  "s_waitcnt vmcnt(4) lgkmcnt(0)\n" \
  "s_barrier\n" \
  "ds_read_b64_tr_b16 v[224:225], v243 offset:16384\n" \
  "ds_read_b64_tr_b16 v[226:227], v243 offset:16896\n" \
  "ds_read_b64_tr_b16 v[228:229], v243 offset:24576\n" \
  "ds_read_b64_tr_b16 v[230:231], v243 offset:25088\n" \
  "s_waitcnt lgkmcnt(0)\n" \
  "s_barrier\n" \
  "ds_write_b128 v242, v[224:227] offset:16384\n" \
  "ds_write_b128 v242, v[228:231] offset:17408\n" \
  "s_waitcnt vmcnt(2) lgkmcnt(0)\n" \
  "s_barrier\n" \
  "ds_read_b64_tr_b16 v[224:225], v243 offset:32768\n" \
  "ds_read_b64_tr_b16 v[226:227], v243 offset:33280\n" \
  "ds_read_b64_tr_b16 v[228:229], v243 offset:40960\n" \
  "ds_read_b64_tr_b16 v[230:231], v243 offset:41472\n" \
  "s_waitcnt lgkmcnt(0)\n" \
  "s_barrier\n" \
  "ds_write_b128 v242, v[224:227] offset:32768\n" \
  "ds_write_b128 v242, v[228:231] offset:33792\n" \
  "s_waitcnt vmcnt(0) lgkmcnt(0)\n" \
  "s_barrier\n" \
  "ds_read_b64_tr_b16 v[224:225], v243 offset:49152\n" \
  "ds_read_b64_tr_b16 v[226:227], v243 offset:49664\n" \
  "ds_read_b64_tr_b16 v[228:229], v243 offset:57344\n" \
  "ds_read_b64_tr_b16 v[230:231], v243 offset:57856\n" \
  "s_waitcnt lgkmcnt(0)\n" \
  "s_barrier\n" \
  "ds_write_b128 v242, v[224:227] offset:49152\n" \
  "ds_write_b128 v242, v[228:231] offset:50176\n" \
  "s_waitcnt lgkmcnt(0)\n" \
  "s_barrier\n" \
  "s_add_u32 s54, s40, RH_TAB\n" \
  "s_addc_u32 s55, s41, 0\n" \
  "s_waitcnt lgkmcnt(0)\n" \
  "s_load_dwordx8 s[64:71], s[54:55], 0x0\n" \
  "s_add_u32 s54, s54, 16\n" \
  "s_addc_u32 s55, s55, 0\n" \
  "s_waitcnt lgkmcnt(0)\n" \
  "s_cmp_eq_u32 s57, 3\n" \
  "s_cselect_b64 s[82:83], -1, 0\n" \
  "s_lshr_b32 s79, s62, 8\n" \
  "s_and_b32 s79, s79, 0xff00\n" \
  "s_and_b32 s62, s62, 0xff00\n" \
  "s_mov_b32 s49, 1440\n" \
  "s_mul_i32 s84, s49, s62\n" \
  "s_lshr_b32 s84, s84, 10\n" \
  "s_mul_i32 s85, s49, s79\n" \
  "s_lshr_b32 s85, s85, 10\n" \
  "s_mul_i32 s86, s49, s63\n" \
  "s_lshr_b32 s86, s86, 10\n" \
  "s_mul_i32 s87, s49, 37\n" \
  "s_lshr_b32 s87, s87, 2\n" \
  "s_add_u32 s87, s87, s61\n" \
  "s_andn2_b32 s87, s87, 0xff\n" \
  "s_add_u32 s46, s58, RH_HINT\n" \
  "s_lshl_b32 s60, s56, 7\n" \
  "s_add_u32 s60, s60, s46\n" \
  "s_cmp_ge_u32 s57, 5\n" \
  "s_cselect_b32 s51, 32, -1\n" \
  "s_cmp_eq_u32 s57, 5\n" \
  "s_cselect_b32 s78, 5, 4\n" \
  "s_mov_b32 s72, s64\n" \
  "s_mov_b32 s73, 0\n" \
  "ds_read_b128 v[176:179], v245\n" \
  "ds_read_b128 v[180:183], v245 offset:1024\n" \
  "v_mov_b64 v[224:225], 0\n" \
  "v_mov_b64 v[226:227], 0\n" \
  "v_mov_b64 v[228:229], 0\n" \
  "v_mov_b64 v[230:231], 0\n" \
  "v_mov_b64 v[232:233], 0\n" \
  "v_mov_b64 v[234:235], 0\n" \
  "v_mov_b64 v[236:237], 0\n" \
  "v_mov_b64 v[238:239], 0\n" \
  "v_subrev_u32 v254, 32768, v245\n" \
  "v_add_u32 v255, 0x10000, v245\n" \
  "s_cmp_ge_u32 s57, 5\n" \
  "s_cbranch_scc1 Ln_b1\n" \
  "s_branch Ln_poll\n" \
  "Ln_fail:\n" \
  "s_waitcnt lgkmcnt(0)\n" \
  "s_add_u32 s73, s73, 1\n" \
  "s_cmp_lt_u32 s73, RH_CAP\n" \
  "s_cbranch_scc1 Ln_poll\n" \
  "v_mov_b32 v0, 1\n" \
  "buffer_store_dword v0, off, s[40:43], 0 offset:1280 sc1\n" \
  "s_endpgm\n" \
  "Ln_poll:\n" \
  "buffer_load_dwordx4 v[184:187], v253, s[40:43], s65 offen sc1\n" \
  "buffer_load_dwordx4 v[188:191], v253, s[40:43], s65 offen offset:1024 sc1\n" \
  "s_waitcnt vmcnt(0)\n" \
  "Ln_chk:\n" \
  "ds_write_b128 v254, v[184:187]\n" \
  "ds_write_b128 v254, v[188:191] offset:1024\n" \
  "v_max3_u32 v192, v184, v185, v186\n" \
  "v_max3_u32 v193, v188, v189, v190\n" \
  "v_max3_u32 v192, v192, v187, v191\n" \
  "v_max_u32 v192, v192, v193\n" \
  "v_cmp_eq_u32 vcc, -1, v192\n" \
  "s_cbranch_vccnz Ln_fail\n" \
  "Ln_b1:\n" \
  "s_bitcmp0_b32 0x60, s57\n" \
  "s_waitcnt lgkmcnt(0)\n" \
  "s_barrier\n" \
  "s_cbranch_scc0 Ln_sp\n" \
  "Ln_nh:\n" \
  "ds_read_b128 v[220:223], v244 offset:9216\n" \
  "ds_read_b128 v[184:187], v244 offset:0\n" \
  "ds_read_b128 v[188:191], v244 offset:1024\n" \
  "s_waitcnt lgkmcnt(2)\n" \
  "v_smfmac_f32_16x16x64_bf16 v[224:227], v[220:223], v[176:183], v246\n" \
  "; MARK7\n" \
  "ds_read_b128 v[192:195], v244 offset:2048\n" \
  "s_waitcnt lgkmcnt(2)\n" \
  "v_smfmac_f32_16x16x64_bf16 v[228:231], v[184:187], v[72:79], v246\n" \
  "ds_read_b128 v[196:199], v244 offset:3072\n" \
  "v_smfmac_f32_16x16x64_bf16 v[224:227], v[184:187], v[0:7], v246\n" \
  "buffer_store_dwordx2 v[216:217], v250, s[40:43], s72 offen sc1\n" \
  "s_waitcnt lgkmcnt(2)\n" \
  "v_smfmac_f32_16x16x64_bf16 v[228:231], v[188:191], v[80:87], v246\n" \
  "ds_read_b128 v[200:203], v244 offset:4096\n" \
  "v_smfmac_f32_16x16x64_bf16 v[224:227], v[188:191], v[8:15], v246\n" \
  "buffer_load_dwordx4 v[240:243], v248, s[40:43], s64 offen sc1\n" \
  "s_waitcnt lgkmcnt(2)\n" \
  "v_smfmac_f32_16x16x64_bf16 v[228:231], v[192:195], v[88:95], v246\n" \
  "ds_read_b128 v[204:207], v244 offset:5120\n" \
  "v_smfmac_f32_16x16x64_bf16 v[224:227], v[192:195], v[16:23], v246\n" \
  "ds_read_b128 v[176:179], v245 offset:16384\n" \
  "ds_read_b128 v[180:183], v245 offset:17408\n" \
  "s_waitcnt lgkmcnt(4)\n" \
  "v_smfmac_f32_16x16x64_bf16 v[228:231], v[196:199], v[96:103], v246\n" \
  "ds_read_b128 v[208:211], v244 offset:6144\n" \
  "v_smfmac_f32_16x16x64_bf16 v[224:227], v[196:199], v[24:31], v246\n" \
  "s_waitcnt lgkmcnt(4)\n" \
  "v_smfmac_f32_16x16x64_bf16 v[228:231], v[200:203], v[104:111], v246\n" \
  "ds_read_b128 v[212:215], v244 offset:7168\n" \
  "v_smfmac_f32_16x16x64_bf16 v[224:227], v[200:203], v[32:39], v246\n" \
  "s_waitcnt lgkmcnt(4)\n" \
  "v_smfmac_f32_16x16x64_bf16 v[228:231], v[204:207], v[112:119], v246\n" \
  "ds_read_b128 v[216:219], v244 offset:8192\n" \
  "v_smfmac_f32_16x16x64_bf16 v[224:227], v[204:207], v[40:47], v246\n" \
  "s_waitcnt lgkmcnt(2)\n" \
  "v_smfmac_f32_16x16x64_bf16 v[224:227], v[208:211], v[48:55], v246\n" \
  "s_waitcnt lgkmcnt(1)\n" \
  "v_smfmac_f32_16x16x64_bf16 v[224:227], v[212:215], v[56:63], v246\n" \
  "s_waitcnt lgkmcnt(0)\n" \
  "v_smfmac_f32_16x16x64_bf16 v[224:227], v[216:219], v[64:71], v246\n" \
  "v_smfmac_f32_16x16x64_bf16 v[228:231], v[208:211], v[120:127], v246\n" \
  "v_smfmac_f32_16x16x64_bf16 v[228:231], v[212:215], v[128:135], v246\n" \
  "v_smfmac_f32_16x16x64_bf16 v[228:231], v[220:223], v[176:183], v246\n" \
  "ds_read_b128 v[176:179], v245 offset:32768\n" \
  "ds_read_b128 v[180:183], v245 offset:33792\n" \
  "v_smfmac_f32_16x16x64_bf16 v[228:231], v[216:219], v[136:143], v246\n" \
  "; MARK6\n" \
  "s_cmp_ge_u32 s57, 4\n" \
  "s_nop 0\n" \
  "v_cvt_pk_bf16_f32 v224, v224, v225\n" \
  "v_cvt_pk_bf16_f32 v225, v226, v227\n" \
  "ds_write_b64 v247, v[224:225] offset:10240\n" \
  "s_nop 2\n" \
  "v_cvt_pk_bf16_f32 v228, v228, v229\n" \
  "v_cvt_pk_bf16_f32 v229, v230, v231\n" \
  "ds_write_b64 v247, v[228:229] offset:14336\n" \
  "s_cbranch_scc0 Ln_o\n" \
  "ds_read_b128 v[224:227], v245 offset:49152\n" \
  "ds_read_b128 v[228:231], v245 offset:50176\n" \
  "s_waitcnt lgkmcnt(2)\n" \
  "s_barrier\n" \
  "ds_read_b32 v254, v244 offset:20224\n" \
  "s_nop 7\n" \
  "s_nop 7\n" \
  "v_smfmac_f32_16x16x64_bf16 v[232:235], v[184:187], v[144:151], v246\n" \
  "v_smfmac_f32_16x16x64_bf16 v[232:235], v[188:191], v[152:159], v246\n" \
  "ds_read_b128 v[184:187], v255 offset:0\n" \
  "ds_read_b128 v[188:191], v255 offset:1024\n" \
  "v_smfmac_f32_16x16x64_bf16 v[232:235], v[192:195], v[160:167], v246\n" \
  "v_smfmac_f32_16x16x64_bf16 v[232:235], v[196:199], v[168:175], v246\n" \
  "ds_read_b128 v[192:195], v255 offset:16384\n" \
  "ds_read_b128 v[196:199], v255 offset:17408\n" \
  "v_smfmac_f32_16x16x64_bf16 v[232:235], v[200:203], v[176:183], v246\n" \
  "ds_read_b128 v[176:179], v255 offset:32768\n" \
  "ds_read_b128 v[180:183], v255 offset:33792\n" \
  "s_waitcnt lgkmcnt(7)\n" \
  "v_smfmac_f32_16x16x64_bf16 v[232:235], v[204:207], v[224:231], v246\n" \
  "ds_read_b128 v[224:227], v255 offset:49152\n" \
  "ds_read_b128 v[228:231], v255 offset:50176\n" \
  "s_waitcnt lgkmcnt(6)\n" \
  "v_smfmac_f32_16x16x64_bf16 v[232:235], v[208:211], v[184:191], v246\n" \
  "s_waitcnt lgkmcnt(4)\n" \
  "v_smfmac_f32_16x16x64_bf16 v[232:235], v[212:215], v[192:199], v246\n" \
  "s_waitcnt lgkmcnt(2)\n" \
  "v_smfmac_f32_16x16x64_bf16 v[232:235], v[216:219], v[176:183], v246\n" \
  "s_waitcnt lgkmcnt(0)\n" \
  "v_smfmac_f32_16x16x64_bf16 v[232:235], v[220:223], v[224:231], v246\n" \
  "s_branch Ln_vd\n" \
  "Ln_o:\n" \
  "s_cmp_eq_u32 s57, 1\n" \
  "s_cbranch_scc1 Ln_va\n" \
  "s_cmp_eq_u32 s57, 2\n" \
  "s_cbranch_scc1 Ln_vb\n" \
  "s_cmp_eq_u32 s57, 3\n" \
  "s_cbranch_scc1 Ln_vp\n" \
  "ds_read_b128 v[224:227], v245 offset:55296\n" \
  "ds_read_b128 v[228:231], v245 offset:56320\n" \
  "s_waitcnt lgkmcnt(2)\n" \
  "s_barrier\n" \
  "ds_read_b32 v254, v244 offset:20224\n" \
  "s_nop 7\n" \
  "s_nop 7\n" \
  "v_smfmac_f32_16x16x64_bf16 v[232:235], v[184:187], v[144:151], v246\n" \
  "v_smfmac_f32_16x16x64_bf16 v[232:235], v[188:191], v[152:159], v246\n" \
  "ds_read_b128 v[184:187], v245 offset:49152\n" \
  "ds_read_b128 v[188:191], v245 offset:50176\n" \
  "v_smfmac_f32_16x16x64_bf16 v[232:235], v[192:195], v[160:167], v246\n" \
  "v_smfmac_f32_16x16x64_bf16 v[232:235], v[196:199], v[168:175], v246\n" \
  "ds_read_b128 v[192:195], v255 offset:6144\n" \
  "ds_read_b128 v[196:199], v255 offset:7168\n" \
  "v_smfmac_f32_16x16x64_bf16 v[232:235], v[200:203], v[176:183], v246\n" \
  "ds_read_b128 v[176:179], v255 offset:0\n" \
  "ds_read_b128 v[180:183], v255 offset:1024\n" \
  "s_waitcnt lgkmcnt(7)\n" \
  "v_smfmac_f32_16x16x64_bf16 v[236:239], v[204:207], v[224:231], v246\n" \
  "ds_read_b128 v[224:227], v255 offset:22528\n" \
  "ds_read_b128 v[228:231], v255 offset:23552\n" \
  "s_waitcnt lgkmcnt(6)\n" \
  "v_smfmac_f32_16x16x64_bf16 v[232:235], v[204:207], v[184:191], v246\n" \
  "ds_read_b128 v[184:187], v255 offset:16384\n" \
  "ds_read_b128 v[188:191], v255 offset:17408\n" \
  "ds_read_b128 v[200:203], v255 offset:38912\n" \
  "ds_read_b128 v[204:207], v255 offset:39936\n" \
  "s_waitcnt lgkmcnt(8)\n" \
  "v_smfmac_f32_16x16x64_bf16 v[236:239], v[208:211], v[192:199], v246\n" \
  "ds_read_b128 v[192:195], v255 offset:55296\n" \
  "ds_read_b128 v[196:199], v255 offset:56320\n" \
  "s_waitcnt lgkmcnt(8)\n" \
  "v_smfmac_f32_16x16x64_bf16 v[232:235], v[208:211], v[176:183], v246\n" \
  "ds_read_b128 v[176:179], v255 offset:32768\n" \
  "ds_read_b128 v[180:183], v255 offset:33792\n" \
  "s_waitcnt lgkmcnt(8)\n" \
  "v_smfmac_f32_16x16x64_bf16 v[236:239], v[212:215], v[224:231], v246\n" \
  "ds_read_b128 v[224:227], v255 offset:49152\n" \
  "ds_read_b128 v[228:231], v255 offset:50176\n" \
  "s_waitcnt lgkmcnt(8)\n" \
  "v_smfmac_f32_16x16x64_bf16 v[232:235], v[212:215], v[184:191], v246\n" \
  "s_waitcnt lgkmcnt(6)\n" \
  "v_smfmac_f32_16x16x64_bf16 v[236:239], v[216:219], v[200:207], v246\n" \
  "s_waitcnt lgkmcnt(4)\n" \
  "v_smfmac_f32_16x16x64_bf16 v[236:239], v[220:223], v[192:199], v246\n" \
  "s_waitcnt lgkmcnt(2)\n" \
  "v_smfmac_f32_16x16x64_bf16 v[232:235], v[216:219], v[176:183], v246\n" \
  "s_waitcnt lgkmcnt(0)\n" \
  "v_smfmac_f32_16x16x64_bf16 v[232:235], v[220:223], v[224:231], v246\n" \
  "v_bfe_u32 v255, v244, 1, 8\n" \
  "v_bfe_u32 v184, v244, 9, 1\n" \
  "v_lshl_add_u32 v255, v184, 2, v255\n" \
  "s_setprio 1\n" \
  "s_nop 0\n" \
  "v_permlane32_swap_b32 v236, v238\n" \
  "v_permlane32_swap_b32 v237, v239\n" \
  "v_add_f32 v236, v236, v238\n" \
  "v_add_f32 v237, v237, v239\n" \
  "v_cvt_pk_bf16_f32 v236, v236, v237\n" \
  "ds_write_b32 v255, v236 offset:19968\n" \
  "s_branch Ln_vs\n" \
  "Ln_va:\n" \
  "ds_read_b128 v[224:227], v245 offset:61440\n" \
  "ds_read_b128 v[228:231], v245 offset:62464\n" \
  "s_waitcnt lgkmcnt(2)\n" \
  "s_barrier\n" \
  "ds_read_b32 v254, v244 offset:20224\n" \
  "s_nop 7\n" \
  "s_nop 7\n" \
  "v_smfmac_f32_16x16x64_bf16 v[232:235], v[184:187], v[144:151], v246\n" \
  "v_smfmac_f32_16x16x64_bf16 v[232:235], v[188:191], v[152:159], v246\n" \
  "ds_read_b128 v[184:187], v245 offset:49152\n" \
  "ds_read_b128 v[188:191], v245 offset:50176\n" \
  "v_smfmac_f32_16x16x64_bf16 v[232:235], v[192:195], v[160:167], v246\n" \
  "v_smfmac_f32_16x16x64_bf16 v[232:235], v[196:199], v[168:175], v246\n" \
  "ds_read_b128 v[192:195], v255 offset:12288\n" \
  "ds_read_b128 v[196:199], v255 offset:13312\n" \
  "v_smfmac_f32_16x16x64_bf16 v[232:235], v[200:203], v[176:183], v246\n" \
  "ds_read_b128 v[176:179], v255 offset:0\n" \
  "ds_read_b128 v[180:183], v255 offset:1024\n" \
  "s_waitcnt lgkmcnt(7)\n" \
  "v_smfmac_f32_16x16x64_bf16 v[236:239], v[204:207], v[224:231], v246\n" \
  "ds_read_b128 v[224:227], v255 offset:28672\n" \
  "ds_read_b128 v[228:231], v255 offset:29696\n" \
  "s_waitcnt lgkmcnt(6)\n" \
  "v_smfmac_f32_16x16x64_bf16 v[232:235], v[204:207], v[184:191], v246\n" \
  "ds_read_b128 v[184:187], v255 offset:16384\n" \
  "ds_read_b128 v[188:191], v255 offset:17408\n" \
  "ds_read_b128 v[200:203], v255 offset:45056\n" \
  "ds_read_b128 v[204:207], v255 offset:46080\n" \
  "s_waitcnt lgkmcnt(8)\n" \
  "v_smfmac_f32_16x16x64_bf16 v[236:239], v[208:211], v[192:199], v246\n" \
  "ds_read_b128 v[192:195], v255 offset:61440\n" \
  "ds_read_b128 v[196:199], v255 offset:62464\n" \
  "s_waitcnt lgkmcnt(8)\n" \
  "v_smfmac_f32_16x16x64_bf16 v[232:235], v[208:211], v[176:183], v246\n" \
  "ds_read_b128 v[176:179], v255 offset:32768\n" \
  "ds_read_b128 v[180:183], v255 offset:33792\n" \
  "s_waitcnt lgkmcnt(8)\n" \
  "v_smfmac_f32_16x16x64_bf16 v[236:239], v[212:215], v[224:231], v246\n" \
  "ds_read_b128 v[224:227], v255 offset:49152\n" \
  "ds_read_b128 v[228:231], v255 offset:50176\n" \
  "s_waitcnt lgkmcnt(8)\n" \
  "v_smfmac_f32_16x16x64_bf16 v[232:235], v[212:215], v[184:191], v246\n" \
  "s_waitcnt lgkmcnt(6)\n" \
  "v_smfmac_f32_16x16x64_bf16 v[236:239], v[216:219], v[200:207], v246\n" \
  "s_waitcnt lgkmcnt(4)\n" \
  "v_smfmac_f32_16x16x64_bf16 v[236:239], v[220:223], v[192:199], v246\n" \
  "s_waitcnt lgkmcnt(2)\n" \
  "v_smfmac_f32_16x16x64_bf16 v[232:235], v[216:219], v[176:183], v246\n" \
  "s_waitcnt lgkmcnt(0)\n" \
  "v_smfmac_f32_16x16x64_bf16 v[232:235], v[220:223], v[224:231], v246\n" \
  "v_bfe_u32 v255, v244, 1, 8\n" \
  "v_bfe_u32 v184, v244, 9, 1\n" \
  "v_lshl_add_u32 v255, v184, 2, v255\n" \
  "s_setprio 1\n" \
  "s_nop 0\n" \
  "v_permlane32_swap_b32 v236, v238\n" \
  "v_permlane32_swap_b32 v237, v239\n" \
  "v_add_f32 v236, v236, v238\n" \
  "v_add_f32 v237, v237, v239\n" \
  "v_cvt_pk_bf16_f32 v236, v236, v237\n" \
  "ds_write_b32 v255, v236 offset:19456\n" \
  "s_branch Ln_vs\n" \
  "Ln_vb:\n" \
  "ds_read_b128 v[224:227], v244 offset:21504\n" \
  "ds_read_b128 v[228:231], v244 offset:22528\n" \
  "s_waitcnt lgkmcnt(2)\n" \
  "s_barrier\n" \
  "ds_read_b32 v254, v244 offset:20224\n" \
  "s_nop 7\n" \
  "s_nop 7\n" \
  "v_smfmac_f32_16x16x64_bf16 v[232:235], v[200:203], v[176:183], v246\n" \
  "ds_read_b128 v[176:179], v244 offset:23552\n" \
  "ds_read_b128 v[180:183], v244 offset:24576\n" \
  "v_smfmac_f32_16x16x64_bf16 v[232:235], v[184:187], v[144:151], v246\n" \
  "s_waitcnt lgkmcnt(3)\n" \
  "v_smfmac_f32_16x16x64_bf16 v[236:239], v[184:187], v[224:231], v246\n" \
  "ds_read_b128 v[224:227], v245 offset:10240\n" \
  "ds_read_b128 v[228:231], v245 offset:11264\n" \
  "v_smfmac_f32_16x16x64_bf16 v[232:235], v[188:191], v[152:159], v246\n" \
  "v_smfmac_f32_16x16x64_bf16 v[232:235], v[192:195], v[160:167], v246\n" \
  "v_smfmac_f32_16x16x64_bf16 v[232:235], v[196:199], v[168:175], v246\n" \
  "s_waitcnt lgkmcnt(2)\n" \
  "v_smfmac_f32_16x16x64_bf16 v[236:239], v[188:191], v[176:183], v246\n" \
  "ds_read_b128 v[176:179], v245 offset:26624\n" \
  "ds_read_b128 v[180:183], v245 offset:27648\n" \
  "ds_read_b128 v[184:187], v245 offset:43008\n" \
  "ds_read_b128 v[188:191], v245 offset:44032\n" \
  "s_waitcnt lgkmcnt(4)\n" \
  "v_smfmac_f32_16x16x64_bf16 v[236:239], v[192:195], v[224:231], v246\n" \
  "ds_read_b128 v[224:227], v245 offset:49152\n" \
  "ds_read_b128 v[228:231], v245 offset:50176\n" \
  "s_waitcnt lgkmcnt(4)\n" \
  "v_smfmac_f32_16x16x64_bf16 v[236:239], v[196:199], v[176:183], v246\n" \
  "ds_read_b128 v[176:179], v255 offset:0\n" \
  "ds_read_b128 v[180:183], v255 offset:1024\n" \
  "ds_read_b128 v[192:195], v255 offset:16384\n" \
  "ds_read_b128 v[196:199], v255 offset:17408\n" \
  "s_waitcnt lgkmcnt(6)\n" \
  "v_smfmac_f32_16x16x64_bf16 v[236:239], v[200:203], v[184:191], v246\n" \
  "ds_read_b128 v[184:187], v255 offset:32768\n" \
  "ds_read_b128 v[188:191], v255 offset:33792\n" \
  "s_waitcnt lgkmcnt(6)\n" \
  "v_smfmac_f32_16x16x64_bf16 v[232:235], v[204:207], v[224:231], v246\n" \
  "ds_read_b128 v[224:227], v255 offset:49152\n" \
  "ds_read_b128 v[228:231], v255 offset:50176\n" \
  "s_waitcnt lgkmcnt(6)\n" \
  "v_smfmac_f32_16x16x64_bf16 v[232:235], v[208:211], v[176:183], v246\n" \
  "s_waitcnt lgkmcnt(4)\n" \
  "v_smfmac_f32_16x16x64_bf16 v[232:235], v[212:215], v[192:199], v246\n" \
  "s_waitcnt lgkmcnt(2)\n" \
  "v_smfmac_f32_16x16x64_bf16 v[232:235], v[216:219], v[184:191], v246\n" \
  "s_waitcnt lgkmcnt(0)\n" \
  "v_smfmac_f32_16x16x64_bf16 v[232:235], v[220:223], v[224:231], v246\n" \
  "v_bfe_u32 v255, v244, 1, 8\n" \
  "v_bfe_u32 v200, v244, 9, 1\n" \
  "v_lshl_add_u32 v255, v200, 2, v255\n" \
  "s_setprio 1\n" \
  "s_nop 0\n" \
  "v_permlane32_swap_b32 v236, v238\n" \
  "v_permlane32_swap_b32 v237, v239\n" \
  "v_add_f32 v236, v236, v238\n" \
  "v_add_f32 v237, v237, v239\n" \
  "v_cvt_pk_bf16_f32 v236, v236, v237\n" \
  "ds_write_b32 v255, v236 offset:19200\n" \
  "s_branch Ln_vs\n" \
  "Ln_vp:\n" \
  "s_waitcnt lgkmcnt(0)\n" \
  "s_barrier\n" \
  "ds_read_b32 v254, v244 offset:20224\n" \
  "s_nop 7\n" \
  "s_nop 7\n" \
  "v_smfmac_f32_16x16x64_bf16 v[232:235], v[184:187], v[144:151], v246\n" \
  "v_smfmac_f32_16x16x64_bf16 v[232:235], v[188:191], v[152:159], v246\n" \
  "v_smfmac_f32_16x16x64_bf16 v[232:235], v[192:195], v[160:167], v246\n" \
  "v_smfmac_f32_16x16x64_bf16 v[232:235], v[196:199], v[168:175], v246\n" \
  "v_smfmac_f32_16x16x64_bf16 v[232:235], v[200:203], v[176:183], v246\n" \
  "s_waitcnt lgkmcnt(0)\n" \
  "v_bfe_u32 v255, v244, 1, 8\n" \
  "v_bfe_u32 v184, v244, 9, 1\n" \
  "v_lshl_add_u32 v255, v184, 2, v255\n" \
  "s_setprio 1\n" \
  "s_nop 3\n" \
  "v_permlane32_swap_b32 v232, v234\n" \
  "v_permlane32_swap_b32 v233, v235\n" \
  "v_add_f32 v232, v232, v234\n" \
  "v_add_f32 v233, v233, v235\n" \
  "v_cvt_pk_bf16_f32 v236, v232, v233\n" \
  "ds_write_b32 v255, v236 offset:19712\n" \
  "s_branch Ln_pa\n" \
  "Ln_pa:\n" \
  "v_lshrrev_b32 v208, 1, v244\n" \
  "s_branch Ln_nk\n" \
  "Ln_vd:\n" \
  "s_setprio 2\n" \
  "s_nop 5\n" \
  "Ln_vs:\n" \
  "v_cvt_pk_bf16_f32 v216, v232, v233\n" \
  "v_cvt_pk_bf16_f32 v217, v234, v235\n" \
  "Ln_nk:\n" \
  "v_readfirstlane_b32 s48, v254\n" \
  "s_and_b32 s48, s48, s51\n" \
  "s_bitcmp1_b32 s48, s78\n" \
  "s_waitcnt vmcnt(0)\n" \
  "s_cbranch_scc1 Ln_ge\n" \
  "Ln_ng:\n" \
  "s_bitcmp1_b32 s48, 3\n" \
  "s_mov_b64 exec, s[82:83]\n" \
  "s_waitcnt lgkmcnt(0)\n" \
  "s_barrier\n" \
  "ds_read_b64 v[216:217], v208 offset:19712\n" \
  "s_mov_b64 exec, -1\n" \
  "ds_read_b128 v[192:195], v251 offset:18432\n" \
  "ds_read_b64 v[196:197], v252\n" \
  "v_mov_b64 v[198:199], 0\n" \
  "s_cbranch_scc1 Ln_gb\n" \
  "Ln_nb:\n" \
  "s_waitcnt lgkmcnt(1)\n" \
  "v_mfma_f32_16x16x32_bf16 v[212:215], v[240:243], v[192:195], 0\n" \
  "s_waitcnt lgkmcnt(0)\n" \
  "v_mfma_f32_16x16x32_bf16 v[212:215], v[216:219], v[196:199], v[212:215]\n" \
  "v_subrev_u32 v254, 32768, v245\n" \
  "v_add_u32 v255, 0x10000, v245\n" \
  "v_mov_b64 v[208:209], -1\n" \
  "s_mov_b32 s73, 0\n" \
  "s_mov_b32 s72, s64\n" \
  "s_bitcmp1_b32 s48, 1\n" \
  "s_nop 1\n" \
  "v_cvt_pk_bf16_f32 v212, v212, v213\n" \
  "v_cvt_pk_bf16_f32 v213, v214, v215\n" \
  "v_min_u32 v212, -2, v212\n" \
  "buffer_store_dwordx2 v[212:213], v249, s[40:43], s65 offen sc1\n" \
  "s_cbranch_scc0 Ln_ne\n" \
  "s_setprio 0\n" \
  "ds_read_b128 v[176:179], v245\n" \
  "ds_read_b128 v[180:183], v245 offset:1024\n" \
  "buffer_store_dwordx2 v[208:209], v249, s[40:43], s66 offen sc1\n" \
  "v_mfma_f32_32x32x8_bf16 v[224:239], v[198:199], v[198:199], 0\n" \
  "s_load_dwordx8 s[64:71], s[54:55], 0x0\n" \
  "s_add_u32 s54, s54, 16\n" \
  "s_addc_u32 s55, s55, 0\n" \
  "s_waitcnt vmcnt(2)\n" \
  "s_branch Ln_chk\n" \
  "Ln_ne:\n" \
  "s_bitcmp1_b32 s48, 2\n" \
  "s_cbranch_scc0 Ln_n2\n" \
  "Ln_g2:\n" \
  "buffer_load_dwordx4 v[184:187], v253, s[40:43], s69 offen sc1\n" \
  "buffer_load_dwordx4 v[188:191], v253, s[40:43], s69 offen offset:1024 sc1\n" \
  "s_setprio 0\n" \
  "ds_read_b128 v[176:179], v245\n" \
  "ds_read_b128 v[180:183], v245 offset:1024\n" \
  "buffer_store_dwordx2 v[208:209], v249, s[40:43], s66 offen sc1\n" \
  "v_mfma_f32_32x32x8_bf16 v[224:239], v[198:199], v[198:199], 0\n" \
  "s_load_dwordx8 s[64:71], s[54:55], 0x0\n" \
  "s_add_u32 s54, s54, 16\n" \
  "s_addc_u32 s55, s55, 0\n" \
  "s_waitcnt vmcnt(1)\n" \
  "s_branch Ln_chk\n" \
  "Ln_n2:\n" \
  "s_setprio 0\n" \
  "s_bitcmp1_b32 s48, 0\n" \
  "s_cbranch_scc0 Ln_g3\n" \
  "v_lshrrev_b32 v184, 2, v244\n" \
  "ds_read_b32 v184, v184 offset:20480\n" \
  "Ln_t2:\n" \
  "s_memrealtime s[44:45]\n" \
  "s_waitcnt lgkmcnt(0)\n" \
  "s_lshl_b32 s74, s44, 8\n" \
  "v_subrev_u32 v185, s74, v184\n" \
  "v_cmp_lt_i32 vcc, 0xff, v185\n" \
  "s_cbranch_vccz Ln_g2\n" \
  "s_add_u32 s73, s73, 1\n" \
  "s_cmp_lt_u32 s73, RH_CAP\n" \
  "s_cbranch_scc1 Ln_t2\n" \
  "v_mov_b32 v0, 1\n" \
  "buffer_store_dword v0, off, s[40:43], 0 offset:1280 sc1\n" \
  "s_endpgm\n" \
  "Ln_g3:\n" \
  "s_cmp_lt_u32 s57, 5\n" \
  "s_cbranch_scc1 Ln_ad\n" \
  "s_memrealtime s[44:45]\n" \
  "s_mul_i32 s84, s49, s62\n" \
  "s_lshr_b32 s84, s84, 10\n" \
  "s_mul_i32 s85, s49, s79\n" \
  "s_lshr_b32 s85, s85, 10\n" \
  "s_mul_i32 s86, s49, s63\n" \
  "s_lshr_b32 s86, s86, 10\n" \
  "s_mul_i32 s87, s49, 37\n" \
  "s_lshr_b32 s87, s87, 2\n" \
  "s_add_u32 s87, s87, s61\n" \
  "s_andn2_b32 s87, s87, 0xff\n" \
  "s_waitcnt lgkmcnt(0)\n" \
  "s_sub_u32 s74, s44, s47\n" \
  "s_lshl_b32 s74, s74, 4\n" \
  "s_sub_u32 s74, s74, s49\n" \
  "s_min_i32 s74, s74, 96\n" \
  "s_ashr_i32 s74, s74, 2\n" \
  "s_add_u32 s49, s49, s74\n" \
  "Ln_ad:\n" \
  "ds_read_b128 v[176:179], v245\n" \
  "ds_read_b128 v[180:183], v245 offset:1024\n" \
  "buffer_store_dwordx2 v[208:209], v249, s[40:43], s66 offen sc1\n" \
  "v_mfma_f32_32x32x8_bf16 v[224:239], v[198:199], v[198:199], 0\n" \
  "s_bitcmp1_b32 s67, 16\n" \
  "s_cbranch_scc0 Ln_df\n" \
  "buffer_store_dwordx2 v[216:217], v250, s[40:43], s72 offen sc1\n" \
  "s_waitcnt vmcnt(0)\n" \
  "s_barrier\n" \
  "s_bitcmp1_b32 s67, 17\n" \
  "s_cbranch_scc0 Ln_df\n" \
  "s_endpgm\n" \
  "Ln_df:\n" \
  "s_load_dwordx8 s[64:71], s[54:55], 0x0\n" \
  "s_add_u32 s54, s54, 16\n" \
  "s_addc_u32 s55, s55, 0\n" \
  "s_cmp_ge_u32 s57, 5\n" \
  "s_cbranch_scc1 Ln_b1\n" \
  "v_lshlrev_b32 v186, 3, v244\n" \
  "v_and_b32 v186, 0xf80, v186\n" \
  "s_waitcnt lgkmcnt(0)\n" \
  "s_and_b32 s74, s67, 0x1000\n" \
  "s_add_u32 s74, s74, s58\n" \
  "s_add_u32 s74, s74, RH_HINT\n" \
  "s_and_b32 s75, s67, 0xff\n" \
  "s_sub_u32 s75, s75, 1\n" \
  "Ln_hp:\n" \
  "buffer_load_dword v184, v186, s[40:43], s74 offen sc1\n" \
  "s_waitcnt vmcnt(0)\n" \
  "v_and_b32 v185, 0xff, v184\n" \
  "v_cmp_lt_u32 vcc, s75, v185\n" \
  "s_cbranch_vccnz Ln_poll\n" \
  "v_cmp_gt_u32 vcc, s75, v185\n" \
  "s_cbranch_vccz Ln_hv\n" \
  "s_add_u32 s73, s73, 1\n" \
  "s_cmp_lt_u32 s73, RH_CAP\n" \
  "s_cbranch_scc1 Ln_hp\n" \
  "v_mov_b32 v0, 1\n" \
  "buffer_store_dword v0, off, s[40:43], 0 offset:1280 sc1\n" \
  "s_endpgm\n" \
  "Ln_hv:\n" \
  "s_andn2_b32 s74, s48, 0xff\n" \
  "v_add_u32 v184, s74, v184\n" \
  "Ln_tw:\n" \
  "s_memrealtime s[44:45]\n" \
  "s_waitcnt lgkmcnt(0)\n" \
  "s_lshl_b32 s74, s44, 8\n" \
  "v_subrev_u32 v185, s74, v184\n" \
  "v_cmp_lt_i32 vcc, 0xff, v185\n" \
  "s_cbranch_vccz Ln_poll\n" \
  "s_add_u32 s73, s73, 1\n" \
  "s_cmp_lt_u32 s73, RH_CAP\n" \
  "s_cbranch_scc1 Ln_tw\n" \
  "v_mov_b32 v0, 1\n" \
  "buffer_store_dword v0, off, s[40:43], 0 offset:1280 sc1\n" \
  "s_endpgm\n" \
  "Ln_sp:\n" \
  "s_cmp_eq_u32 s57, 6\n" \
  "s_cbranch_scc1 Ln_h6\n" \
  "s_memrealtime s[44:45]\n" \
  "s_and_b32 s75, s71, 0xff\n" \
  "s_sub_u32 s75, s75, 1\n" \
  "v_lshrrev_b32 v204, 2, v244\n" \
  "s_waitcnt vmcnt(2)\n" \
  "v_and_b32 v254, 0xff, v253\n" \
  "v_add_u32 v253, s87, v253\n" \
  "v_cmp_ne_u32 s[52:53], s75, v254\n" \
  "s_waitcnt lgkmcnt(0)\n" \
  "ds_write_b32 v204, v253 offset:20480\n" \
  "s_lshl_b32 s74, s44, 8\n" \
  "s_mov_b32 s47, s44\n" \
  "v_subrev_u32 v254, s74, v253\n" \
  "v_cmp_le_i32 vcc, s86, v254\n" \
  "s_cmp_eq_u64 vcc, 0\n" \
  "s_cselect_b32 s48, 5, 1\n" \
  "v_cmp_le_i32 vcc, s85, v254\n" \
  "s_cmp_eq_u64 vcc, 0\n" \
  "s_cselect_b32 s48, 11, s48\n" \
  "v_cmp_le_i32 vcc, s84, v254\n" \
  "s_cmp_eq_u64 vcc, 0\n" \
  "s_cselect_b32 s48, 19, s48\n" \
  "s_cmp_eq_u64 s[52:53], 0\n" \
  "s_cselect_b32 s48, s48, 0\n" \
  "s_cmp_eq_u32 s75, 0\n" \
  "s_cselect_b32 s48, 19, s48\n" \
  "s_bitcmp1_b32 s67, 16\n" \
  "s_cselect_b32 s48, 0, s48\n" \
  "s_or_b32 s48, s48, 32\n" \
  "s_or_b32 s48, s48, s87\n" \
  "v_mov_b32 v254, s48\n" \
  "ds_write_b32 v204, v254 offset:20224\n" \
  "s_branch Ln_nh\n" \
  "Ln_h6:\n" \
  "s_memrealtime s[44:45]\n" \
  "s_and_b32 s75, s67, 0x1000\n" \
  "s_add_u32 s75, s75, s60\n" \
  "s_and_b32 s77, s67, 0xff\n" \
  "s_lshr_b32 s50, s49, 4\n" \
  "s_add_u32 s50, s50, s59\n" \
  "s_waitcnt lgkmcnt(0)\n" \
  "s_mov_b32 s47, s44\n" \
  "s_add_u32 s74, s44, s50\n" \
  "s_lshl_b32 s74, s74, 8\n" \
  "s_or_b32 s74, s74, s77\n" \
  "v_mov_b32 v254, s74\n" \
  "s_mov_b64 exec, 1\n" \
  "buffer_store_dword v254, off, s[40:43], s75 sc1\n" \
  "s_mov_b64 exec, -1\n" \
  "s_branch Ln_nh\n" \
  "Ln_ge:\n" \
  "s_cmp_eq_u32 s57, 5\n" \
  "s_cbranch_scc1 Ln_g5\n" \
  "buffer_load_dwordx4 v[184:187], v253, s[40:43], s69 offen sc1\n" \
  "buffer_load_dwordx4 v[188:191], v253, s[40:43], s69 offen offset:1024 sc1\n" \
  "s_branch Ln_ng\n" \
  "Ln_g5:\n" \
  "v_lshlrev_b32 v253, 3, v244\n" \
  "s_and_b32 s74, s67, 0x1000\n" \
  "v_and_b32 v253, 0xf80, v253\n" \
  "s_add_u32 s74, s74, s46\n" \
  "buffer_load_dword v253, v253, s[40:43], s74 offen sc1\n" \
  "s_branch Ln_ng\n" \
  "Ln_gb:\n" \
  "buffer_load_dwordx4 v[184:187], v253, s[40:43], s69 offen sc1\n" \
  "buffer_load_dwordx4 v[188:191], v253, s[40:43], s69 offen offset:1024 sc1\n" \
  "s_branch Ln_nb\n"
#define RH_ASM_A \
  "v_xor_b32 v242, 64, v240\n" \
  "v_add_u32 v242, 31744, v242\n" \
  "v_add_u32 v243, 0x10000, v241\n" \
  "s_add_u32 m0, s78, 32768\n" \
  "s_add_u32 s74, s79, 0\n" \
  "buffer_load_dwordx4 v240, s[44:47], s74 offen lds\n" \
  "buffer_load_dwordx4 v242, s[44:47], s74 offen offset:1024 lds\n" \
  "s_add_u32 m0, s78, 49152\n" \
  "s_add_u32 s74, s79, 524288\n" \
  "buffer_load_dwordx4 v240, s[44:47], s74 offen lds\n" \
  "buffer_load_dwordx4 v242, s[44:47], s74 offen offset:1024 lds\n" \
  "s_add_u32 m0, s78, 65536\n" \
  "s_add_u32 s74, s79, 1048576\n" \
  "buffer_load_dwordx4 v240, s[44:47], s74 offen lds\n" \
  "buffer_load_dwordx4 v242, s[44:47], s74 offen offset:1024 lds\n" \
  "s_add_u32 m0, s78, 81920\n" \
  "s_add_u32 s74, s79, 1572864\n" \
  "buffer_load_dwordx4 v240, s[44:47], s74 offen lds\n" \
  "buffer_load_dwordx4 v242, s[44:47], s74 offen offset:1024 lds\n" \
  "s_add_u32 m0, s78, 98304\n" \
  "s_add_u32 s74, s79, 2097152\n" \
  "buffer_load_dwordx4 v240, s[44:47], s74 offen lds\n" \
  "buffer_load_dwordx4 v242, s[44:47], s74 offen offset:1024 lds\n" \
  "s_add_u32 m0, s78, 114688\n" \
  "s_add_u32 s74, s79, 2621440\n" \
  "buffer_load_dwordx4 v240, s[44:47], s74 offen lds\n" \
  "buffer_load_dwordx4 v242, s[44:47], s74 offen offset:1024 lds\n" \
  "s_add_u32 m0, s78, 0\n" \
  "s_add_u32 s74, s79, 3145728\n" \
  "buffer_load_dwordx4 v240, s[44:47], s74 offen lds\n" \
  "buffer_load_dwordx4 v242, s[44:47], s74 offen offset:1024 lds\n" \
  "s_add_u32 m0, s78, 16384\n" \
  "s_add_u32 s74, s79, 3670016\n" \
  "buffer_load_dwordx4 v240, s[44:47], s74 offen lds\n" \
  "buffer_load_dwordx4 v242, s[44:47], s74 offen offset:1024 lds\n" \
  "s_waitcnt vmcnt(14) lgkmcnt(0)\n" \
  "s_barrier\n" \
  "ds_read_b64_tr_b16 v[0:1], v241 offset:32768\n" \
  "ds_read_b64_tr_b16 v[2:3], v241 offset:33280\n" \
  "ds_read_b64_tr_b16 v[4:5], v241 offset:40960\n" \
  "ds_read_b64_tr_b16 v[6:7], v241 offset:41472\n" \
  "s_waitcnt lgkmcnt(0)\n" \
  "s_barrier\n" \
  "s_add_u32 m0, s78, 32768\n" \
  "s_add_u32 s74, s79, 4194304\n" \
  "buffer_load_dwordx4 v240, s[44:47], s74 offen lds\n" \
  "buffer_load_dwordx4 v242, s[44:47], s74 offen offset:1024 lds\n" \
  "s_waitcnt vmcnt(14) lgkmcnt(0)\n" \
  "s_barrier\n" \
  "ds_read_b64_tr_b16 v[8:9], v241 offset:49152\n" \
  "ds_read_b64_tr_b16 v[10:11], v241 offset:49664\n" \
  "ds_read_b64_tr_b16 v[12:13], v241 offset:57344\n" \
  "ds_read_b64_tr_b16 v[14:15], v241 offset:57856\n" \
  "s_waitcnt lgkmcnt(0)\n" \
  "s_barrier\n" \
  "s_add_u32 m0, s78, 49152\n" \
  "s_add_u32 s74, s79, 0\n" \
  "buffer_load_dwordx4 v240, s[48:51], s74 offen lds\n" \
  "buffer_load_dwordx4 v242, s[48:51], s74 offen offset:1024 lds\n" \
  "s_waitcnt vmcnt(14) lgkmcnt(0)\n" \
  "s_barrier\n" \
  "ds_read_b64_tr_b16 v[16:17], v243 offset:0\n" \
  "ds_read_b64_tr_b16 v[18:19], v243 offset:512\n" \
  "ds_read_b64_tr_b16 v[20:21], v243 offset:8192\n" \
  "ds_read_b64_tr_b16 v[22:23], v243 offset:8704\n" \
  "s_waitcnt lgkmcnt(0)\n" \
  "s_barrier\n" \
  "s_add_u32 m0, s78, 65536\n" \
  "s_add_u32 s74, s79, 524288\n" \
  "buffer_load_dwordx4 v240, s[48:51], s74 offen lds\n" \
  "buffer_load_dwordx4 v242, s[48:51], s74 offen offset:1024 lds\n" \
  "s_waitcnt vmcnt(14) lgkmcnt(0)\n" \
  "s_barrier\n" \
  "ds_read_b64_tr_b16 v[24:25], v243 offset:16384\n" \
  "ds_read_b64_tr_b16 v[26:27], v243 offset:16896\n" \
  "ds_read_b64_tr_b16 v[28:29], v243 offset:24576\n" \
  "ds_read_b64_tr_b16 v[30:31], v243 offset:25088\n" \
  "s_waitcnt lgkmcnt(0)\n" \
  "s_barrier\n" \
  "s_add_u32 m0, s78, 81920\n" \
  "s_add_u32 s74, s79, 1048576\n" \
  "buffer_load_dwordx4 v240, s[48:51], s74 offen lds\n" \
  "buffer_load_dwordx4 v242, s[48:51], s74 offen offset:1024 lds\n" \
  "s_waitcnt vmcnt(14) lgkmcnt(0)\n" \
  "s_barrier\n" \
  "ds_read_b64_tr_b16 v[32:33], v243 offset:32768\n" \
  "ds_read_b64_tr_b16 v[34:35], v243 offset:33280\n" \
  "ds_read_b64_tr_b16 v[36:37], v243 offset:40960\n" \
  "ds_read_b64_tr_b16 v[38:39], v243 offset:41472\n" \
  "s_waitcnt lgkmcnt(0)\n" \
  "s_barrier\n" \
  "s_add_u32 m0, s78, 98304\n" \
  "s_add_u32 s74, s79, 1572864\n" \
  "buffer_load_dwordx4 v240, s[48:51], s74 offen lds\n" \
  "buffer_load_dwordx4 v242, s[48:51], s74 offen offset:1024 lds\n" \
  "s_waitcnt vmcnt(14) lgkmcnt(0)\n" \
  "s_barrier\n" \
  "ds_read_b64_tr_b16 v[40:41], v243 offset:49152\n" \
  "ds_read_b64_tr_b16 v[42:43], v243 offset:49664\n" \
  "ds_read_b64_tr_b16 v[44:45], v243 offset:57344\n" \
  "ds_read_b64_tr_b16 v[46:47], v243 offset:57856\n" \
  "s_waitcnt lgkmcnt(0)\n" \
  "s_barrier\n" \
  "s_add_u32 m0, s78, 114688\n" \
  "s_add_u32 s74, s79, 2097152\n" \
  "buffer_load_dwordx4 v240, s[48:51], s74 offen lds\n" \
  "buffer_load_dwordx4 v242, s[48:51], s74 offen offset:1024 lds\n" \
  "s_waitcnt vmcnt(14) lgkmcnt(0)\n" \
  "s_barrier\n" \
  "ds_read_b64_tr_b16 v[48:49], v241 offset:0\n" \
  "ds_read_b64_tr_b16 v[50:51], v241 offset:512\n" \
  "ds_read_b64_tr_b16 v[52:53], v241 offset:8192\n" \
  "ds_read_b64_tr_b16 v[54:55], v241 offset:8704\n" \
  "s_waitcnt lgkmcnt(0)\n" \
  "s_barrier\n" \
  "s_add_u32 m0, s78, 0\n" \
  "s_add_u32 s74, s79, 2621440\n" \
  "buffer_load_dwordx4 v240, s[48:51], s74 offen lds\n" \
  "buffer_load_dwordx4 v242, s[48:51], s74 offen offset:1024 lds\n" \
  "s_waitcnt vmcnt(14) lgkmcnt(0)\n" \
  "s_barrier\n" \
  "ds_read_b64_tr_b16 v[56:57], v241 offset:16384\n" \
  "ds_read_b64_tr_b16 v[58:59], v241 offset:16896\n" \
  "ds_read_b64_tr_b16 v[60:61], v241 offset:24576\n" \
  "ds_read_b64_tr_b16 v[62:63], v241 offset:25088\n" \
  "s_waitcnt lgkmcnt(0)\n" \
  "s_barrier\n" \
  "s_add_u32 m0, s78, 16384\n" \
  "s_add_u32 s74, s79, 3145728\n" \
  "buffer_load_dwordx4 v240, s[48:51], s74 offen lds\n" \
  "buffer_load_dwordx4 v242, s[48:51], s74 offen offset:1024 lds\n" \
  "s_waitcnt vmcnt(14) lgkmcnt(0)\n" \
  "s_barrier\n" \
  "ds_read_b64_tr_b16 v[64:65], v241 offset:32768\n" \
  "ds_read_b64_tr_b16 v[66:67], v241 offset:33280\n" \
  "ds_read_b64_tr_b16 v[68:69], v241 offset:40960\n" \
  "ds_read_b64_tr_b16 v[70:71], v241 offset:41472\n" \
  "s_waitcnt lgkmcnt(0)\n" \
  "s_barrier\n" \
  "s_add_u32 m0, s78, 32768\n" \
  "s_add_u32 s74, s79, 3670016\n" \
  "buffer_load_dwordx4 v240, s[48:51], s74 offen lds\n" \
  "buffer_load_dwordx4 v242, s[48:51], s74 offen offset:1024 lds\n" \
  "s_waitcnt vmcnt(14) lgkmcnt(0)\n" \
  "s_barrier\n" \
  "ds_read_b64_tr_b16 v[80:81], v241 offset:49152\n" \
  "ds_read_b64_tr_b16 v[82:83], v241 offset:49664\n" \
  "ds_read_b64_tr_b16 v[84:85], v241 offset:57344\n" \
  "ds_read_b64_tr_b16 v[86:87], v241 offset:57856\n" \
  "s_waitcnt lgkmcnt(0)\n" \
  "s_barrier\n" \
  "s_add_u32 m0, s78, 49152\n" \
  "s_add_u32 s74, s79, 4194304\n" \
  "buffer_load_dwordx4 v240, s[48:51], s74 offen lds\n" \
  "buffer_load_dwordx4 v242, s[48:51], s74 offen offset:1024 lds\n" \
  "s_waitcnt vmcnt(14) lgkmcnt(0)\n" \
  "s_barrier\n" \
  "ds_read_b64_tr_b16 v[88:89], v243 offset:0\n" \
  "ds_read_b64_tr_b16 v[90:91], v243 offset:512\n" \
  "ds_read_b64_tr_b16 v[92:93], v243 offset:8192\n" \
  "ds_read_b64_tr_b16 v[94:95], v243 offset:8704\n" \
  "s_waitcnt lgkmcnt(0)\n" \
  "s_barrier\n" \
  "s_add_u32 m0, s78, 65536\n" \
  "s_add_u32 s74, s79, 0\n" \
  "buffer_load_dwordx4 v240, s[52:55], s74 offen lds\n" \
  "buffer_load_dwordx4 v242, s[52:55], s74 offen offset:1024 lds\n" \
  "s_waitcnt vmcnt(14) lgkmcnt(0)\n" \
  "s_barrier\n" \
  "ds_read_b64_tr_b16 v[96:97], v243 offset:16384\n" \
  "ds_read_b64_tr_b16 v[98:99], v243 offset:16896\n" \
  "ds_read_b64_tr_b16 v[100:101], v243 offset:24576\n" \
  "ds_read_b64_tr_b16 v[102:103], v243 offset:25088\n" \
  "s_waitcnt lgkmcnt(0)\n" \
  "s_barrier\n" \
  "s_add_u32 m0, s78, 81920\n" \
  "s_add_u32 s74, s79, 524288\n" \
  "buffer_load_dwordx4 v240, s[52:55], s74 offen lds\n" \
  "buffer_load_dwordx4 v242, s[52:55], s74 offen offset:1024 lds\n" \
  "s_waitcnt vmcnt(14) lgkmcnt(0)\n" \
  "s_barrier\n" \
  "ds_read_b64_tr_b16 v[104:105], v243 offset:32768\n" \
  "ds_read_b64_tr_b16 v[106:107], v243 offset:33280\n" \
  "ds_read_b64_tr_b16 v[108:109], v243 offset:40960\n" \
  "ds_read_b64_tr_b16 v[110:111], v243 offset:41472\n" \
  "s_waitcnt lgkmcnt(0)\n" \
  "s_barrier\n" \
  "s_add_u32 m0, s78, 98304\n" \
  "s_add_u32 s74, s79, 1048576\n" \
  "buffer_load_dwordx4 v240, s[52:55], s74 offen lds\n" \
  "buffer_load_dwordx4 v242, s[52:55], s74 offen offset:1024 lds\n" \
  "s_waitcnt vmcnt(14) lgkmcnt(0)\n" \
  "s_barrier\n" \
  "ds_read_b64_tr_b16 v[112:113], v243 offset:49152\n" \
  "ds_read_b64_tr_b16 v[114:115], v243 offset:49664\n" \
  "ds_read_b64_tr_b16 v[116:117], v243 offset:57344\n" \
  "ds_read_b64_tr_b16 v[118:119], v243 offset:57856\n" \
  "s_waitcnt lgkmcnt(0)\n" \
  "s_barrier\n" \
  "s_add_u32 m0, s78, 114688\n" \
  "s_add_u32 s74, s79, 1572864\n" \
  "buffer_load_dwordx4 v240, s[52:55], s74 offen lds\n" \
  "buffer_load_dwordx4 v242, s[52:55], s74 offen offset:1024 lds\n" \
  "s_waitcnt vmcnt(14) lgkmcnt(0)\n" \
  "s_barrier\n" \
  "ds_read_b64_tr_b16 v[120:121], v241 offset:0\n" \
  "ds_read_b64_tr_b16 v[122:123], v241 offset:512\n" \
  "ds_read_b64_tr_b16 v[124:125], v241 offset:8192\n" \
  "ds_read_b64_tr_b16 v[126:127], v241 offset:8704\n" \
  "s_waitcnt lgkmcnt(0)\n" \
  "s_barrier\n" \
  "s_add_u32 m0, s78, 0\n" \
  "s_add_u32 s74, s79, 4718592\n" \
  "buffer_load_dwordx4 v240, s[44:47], s74 offen lds\n" \
  "buffer_load_dwordx4 v242, s[44:47], s74 offen offset:1024 lds\n" \
  "s_waitcnt vmcnt(14) lgkmcnt(0)\n" \
  "s_barrier\n" \
  "ds_read_b64_tr_b16 v[128:129], v241 offset:16384\n" \
  "ds_read_b64_tr_b16 v[130:131], v241 offset:16896\n" \
  "ds_read_b64_tr_b16 v[132:133], v241 offset:24576\n" \
  "ds_read_b64_tr_b16 v[134:135], v241 offset:25088\n" \
  "s_waitcnt lgkmcnt(0)\n" \
  "s_barrier\n" \
  "s_add_u32 m0, s78, 16384\n" \
  "s_add_u32 s74, s79, 4718592\n" \
  "buffer_load_dwordx4 v240, s[48:51], s74 offen lds\n" \
  "buffer_load_dwordx4 v242, s[48:51], s74 offen offset:1024 lds\n" \
  "s_waitcnt vmcnt(14) lgkmcnt(0)\n" \
  "s_barrier\n" \
  "ds_read_b64_tr_b16 v[136:137], v241 offset:32768\n" \
  "ds_read_b64_tr_b16 v[138:139], v241 offset:33280\n" \
  "ds_read_b64_tr_b16 v[140:141], v241 offset:40960\n" \
  "ds_read_b64_tr_b16 v[142:143], v241 offset:41472\n" \
  "s_waitcnt lgkmcnt(0)\n" \
  "s_barrier\n" \
  "s_add_u32 m0, s78, 32768\n" \
  "s_add_u32 s74, s79, 2097152\n" \
  "buffer_load_dwordx4 v240, s[52:55], s74 offen lds\n" \
  "buffer_load_dwordx4 v242, s[52:55], s74 offen offset:1024 lds\n" \
  "s_waitcnt vmcnt(14) lgkmcnt(0)\n" \
  "s_barrier\n" \
  "ds_read_b64_tr_b16 v[144:145], v241 offset:49152\n" \
  "ds_read_b64_tr_b16 v[146:147], v241 offset:49664\n" \
  "ds_read_b64_tr_b16 v[148:149], v241 offset:57344\n" \
  "ds_read_b64_tr_b16 v[150:151], v241 offset:57856\n" \
  "s_waitcnt lgkmcnt(0)\n" \
  "s_barrier\n" \
  "s_add_u32 m0, s78, 49152\n" \
  "s_add_u32 s74, s79, 2621440\n" \
  "buffer_load_dwordx4 v240, s[52:55], s74 offen lds\n" \
  "buffer_load_dwordx4 v242, s[52:55], s74 offen offset:1024 lds\n" \
  "s_waitcnt vmcnt(14) lgkmcnt(0)\n" \
  "s_barrier\n" \
  "ds_read_b64_tr_b16 v[224:225], v243 offset:0\n" \
  "ds_read_b64_tr_b16 v[226:227], v243 offset:512\n" \
  "ds_read_b64_tr_b16 v[228:229], v243 offset:8192\n" \
  "ds_read_b64_tr_b16 v[230:231], v243 offset:8704\n" \
  "s_waitcnt lgkmcnt(0)\n" \
  "s_barrier\n" \
  "ds_write_b128 v244, v[224:227] offset:21504\n" \
  "ds_write_b128 v244, v[228:231] offset:22528\n" \
  "s_add_u32 m0, s78, 65536\n" \
  "s_add_u32 s74, s79, 3145728\n" \
  "buffer_load_dwordx4 v240, s[52:55], s74 offen lds\n" \
  "buffer_load_dwordx4 v242, s[52:55], s74 offen offset:1024 lds\n" \
  "s_waitcnt vmcnt(14) lgkmcnt(0)\n" \
  "s_barrier\n" \
  "ds_read_b64_tr_b16 v[224:225], v243 offset:16384\n" \
  "ds_read_b64_tr_b16 v[226:227], v243 offset:16896\n" \
  "ds_read_b64_tr_b16 v[228:229], v243 offset:24576\n" \
  "ds_read_b64_tr_b16 v[230:231], v243 offset:25088\n" \
  "s_waitcnt lgkmcnt(0)\n" \
  "s_barrier\n" \
  "ds_write_b128 v244, v[224:227] offset:23552\n" \
  "ds_write_b128 v244, v[228:231] offset:24576\n" \
  "s_add_u32 m0, s78, 81920\n" \
  "s_add_u32 s74, s79, 3670016\n" \
  "buffer_load_dwordx4 v240, s[52:55], s74 offen lds\n" \
  "buffer_load_dwordx4 v242, s[52:55], s74 offen offset:1024 lds\n" \
  "s_waitcnt vmcnt(14) lgkmcnt(0)\n" \
  "s_barrier\n" \
  "ds_read_b64_tr_b16 v[160:161], v243 offset:32768\n" \
  "ds_read_b64_tr_b16 v[162:163], v243 offset:33280\n" \
  "ds_read_b64_tr_b16 v[164:165], v243 offset:40960\n" \
  "ds_read_b64_tr_b16 v[166:167], v243 offset:41472\n" \
  "s_waitcnt lgkmcnt(0)\n" \
  "s_barrier\n" \
  "s_add_u32 m0, s78, 98304\n" \
  "s_add_u32 s74, s79, 4194304\n" \
  "buffer_load_dwordx4 v240, s[52:55], s74 offen lds\n" \
  "buffer_load_dwordx4 v242, s[52:55], s74 offen offset:1024 lds\n" \
  "s_waitcnt vmcnt(14) lgkmcnt(0)\n" \
  "s_barrier\n" \
  "ds_read_b64_tr_b16 v[168:169], v243 offset:49152\n" \
  "ds_read_b64_tr_b16 v[170:171], v243 offset:49664\n" \
  "ds_read_b64_tr_b16 v[172:173], v243 offset:57344\n" \
  "ds_read_b64_tr_b16 v[174:175], v243 offset:57856\n" \
  "s_waitcnt lgkmcnt(0)\n" \
  "s_barrier\n" \
  "s_add_u32 m0, s78, 114688\n" \
  "s_add_u32 s74, s79, 4718592\n" \
  "buffer_load_dwordx4 v240, s[52:55], s74 offen lds\n" \
  "buffer_load_dwordx4 v242, s[52:55], s74 offen offset:1024 lds\n" \
  "s_waitcnt vmcnt(14) lgkmcnt(0)\n" \
  "s_barrier\n" \
  "ds_read_b64_tr_b16 v[72:73], v241 offset:0\n" \
  "ds_read_b64_tr_b16 v[74:75], v241 offset:512\n" \
  "ds_read_b64_tr_b16 v[76:77], v241 offset:8192\n" \
  "ds_read_b64_tr_b16 v[78:79], v241 offset:8704\n" \
  "s_waitcnt lgkmcnt(0)\n" \
  "s_barrier\n" \
  "ds_write_b128 v245, v[160:163] offset:0\n" \
  "ds_write_b128 v245, v[164:167] offset:1024\n" \
  "s_waitcnt vmcnt(12) lgkmcnt(0)\n" \
  "s_barrier\n" \
  "ds_read_b64_tr_b16 v[152:153], v241 offset:16384\n" \
  "ds_read_b64_tr_b16 v[154:155], v241 offset:16896\n" \
  "ds_read_b64_tr_b16 v[156:157], v241 offset:24576\n" \
  "ds_read_b64_tr_b16 v[158:159], v241 offset:25088\n" \
  "s_waitcnt lgkmcnt(0)\n" \
  "s_barrier\n" \
  "ds_write_b128 v245, v[168:171] offset:16384\n" \
  "ds_write_b128 v245, v[172:175] offset:17408\n" \
  "s_waitcnt vmcnt(10) lgkmcnt(0)\n" \
  "s_barrier\n" \
  "ds_read_b64_tr_b16 v[224:225], v241 offset:32768\n" \
  "ds_read_b64_tr_b16 v[226:227], v241 offset:33280\n" \
  "ds_read_b64_tr_b16 v[228:229], v241 offset:40960\n" \
  "ds_read_b64_tr_b16 v[230:231], v241 offset:41472\n" \
  "s_waitcnt lgkmcnt(0)\n" \
  "s_barrier\n" \
  "ds_write_b128 v245, v[224:227] offset:32768\n" \
  "ds_write_b128 v245, v[228:231] offset:33792\n" \
  "s_waitcnt vmcnt(8) lgkmcnt(0)\n" \
  "s_barrier\n" \
  "ds_read_b64_tr_b16 v[224:225], v241 offset:49152\n" \
  "ds_read_b64_tr_b16 v[226:227], v241 offset:49664\n" \
  "ds_read_b64_tr_b16 v[228:229], v241 offset:57344\n" \
  "ds_read_b64_tr_b16 v[230:231], v241 offset:57856\n" \
  "s_waitcnt lgkmcnt(0)\n" \
  "s_barrier\n" \
  "ds_write_b128 v245, v[224:227] offset:49152\n" \
  "ds_write_b128 v245, v[228:231] offset:50176\n" \
  "s_waitcnt vmcnt(6) lgkmcnt(0)\n" \
  "s_barrier\n" \
  "ds_read_b64_tr_b16 v[224:225], v243 offset:0\n" \
  "ds_read_b64_tr_b16 v[226:227], v243 offset:512\n" \
  "ds_read_b64_tr_b16 v[228:229], v243 offset:8192\n" \
  "ds_read_b64_tr_b16 v[230:231], v243 offset:8704\n" \
  "s_waitcnt lgkmcnt(0)\n" \
  "s_barrier\n" \
  "v_add_u32 v242, 0x10000, v245\n" \
  "ds_write_b128 v242, v[224:227] offset:0\n" \
  "ds_write_b128 v242, v[228:231] offset:1024\n" \
  "s_waitcnt vmcnt(4) lgkmcnt(0)\n" \
  "s_barrier\n" \
  "ds_read_b64_tr_b16 v[224:225], v243 offset:16384\n" \
  "ds_read_b64_tr_b16 v[226:227], v243 offset:16896\n" \
  "ds_read_b64_tr_b16 v[228:229], v243 offset:24576\n" \
  "ds_read_b64_tr_b16 v[230:231], v243 offset:25088\n" \
  "s_waitcnt lgkmcnt(0)\n" \
  "s_barrier\n" \
  "ds_write_b128 v242, v[224:227] offset:16384\n" \
  "ds_write_b128 v242, v[228:231] offset:17408\n" \
  "s_waitcnt vmcnt(2) lgkmcnt(0)\n" \
  "s_barrier\n" \
  "ds_read_b64_tr_b16 v[224:225], v243 offset:32768\n" \
  "ds_read_b64_tr_b16 v[226:227], v243 offset:33280\n" \
  "ds_read_b64_tr_b16 v[228:229], v243 offset:40960\n" \
  "ds_read_b64_tr_b16 v[230:231], v243 offset:41472\n" \
  "s_waitcnt lgkmcnt(0)\n" \
  "s_barrier\n" \
  "ds_write_b128 v242, v[224:227] offset:32768\n" \
  "ds_write_b128 v242, v[228:231] offset:33792\n" \
  "s_waitcnt vmcnt(0) lgkmcnt(0)\n" \
  "s_barrier\n" \
  "ds_read_b64_tr_b16 v[224:225], v243 offset:49152\n" \
  "ds_read_b64_tr_b16 v[226:227], v243 offset:49664\n" \
  "ds_read_b64_tr_b16 v[228:229], v243 offset:57344\n" \
  "ds_read_b64_tr_b16 v[230:231], v243 offset:57856\n" \
  "s_waitcnt lgkmcnt(0)\n" \
  "s_barrier\n" \
  "ds_write_b128 v242, v[224:227] offset:49152\n" \
  "ds_write_b128 v242, v[228:231] offset:50176\n" \
  "s_waitcnt lgkmcnt(0)\n" \
  "s_barrier\n" \
  "s_add_u32 s54, s40, RH_TAB\n" \
  "s_addc_u32 s55, s41, 0\n" \
  "s_load_dword s82, s[54:55], 0x0\n" \
  "s_load_dword s83, s[54:55], 0x40\n" \
  "s_load_dword s84, s[54:55], 0x80\n" \
  "s_load_dword s85, s[54:55], 0xc0\n" \
  "s_load_dword s86, s[54:55], 0x100\n" \
  "s_load_dword s87, s[54:55], 0x140\n" \
  "s_load_dword s88, s[54:55], 0x180\n" \
  "s_load_dword s89, s[54:55], 0x1c0\n" \
  "s_waitcnt lgkmcnt(0)\n" \
  "s_load_dword s82, s[54:55], 0x200\n" \
  "s_load_dword s83, s[54:55], 0x240\n" \
  "s_load_dword s84, s[54:55], 0x280\n" \
  "s_load_dword s85, s[54:55], 0x2c0\n" \
  "s_load_dword s86, s[54:55], 0x300\n" \
  "s_load_dword s87, s[54:55], 0x340\n" \
  "s_load_dword s88, s[54:55], 0x380\n" \
  "s_load_dword s89, s[54:55], 0x3c0\n" \
  "s_waitcnt lgkmcnt(0)\n" \
  "s_load_dword s82, s[54:55], 0x400\n" \
  "s_load_dword s83, s[54:55], 0x440\n" \
  "s_load_dword s84, s[54:55], 0x480\n" \
  "s_load_dword s85, s[54:55], 0x4c0\n" \
  "s_load_dword s86, s[54:55], 0x500\n" \
  "s_load_dword s87, s[54:55], 0x540\n" \
  "s_load_dword s88, s[54:55], 0x580\n" \
  "s_load_dword s89, s[54:55], 0x5c0\n" \
  "s_waitcnt lgkmcnt(0)\n" \
  "s_load_dword s82, s[54:55], 0x600\n" \
  "s_load_dword s83, s[54:55], 0x640\n" \
  "s_load_dword s84, s[54:55], 0x680\n" \
  "s_load_dword s85, s[54:55], 0x6c0\n" \
  "s_load_dword s86, s[54:55], 0x700\n" \
  "s_load_dword s87, s[54:55], 0x740\n" \
  "s_load_dword s88, s[54:55], 0x780\n" \
  "s_load_dword s89, s[54:55], 0x7c0\n" \
  "s_waitcnt lgkmcnt(0)\n" \
  "s_load_dword s82, s[54:55], 0x800\n" \
  "s_waitcnt lgkmcnt(0)\n" \
  "s_load_dwordx8 s[64:71], s[54:55], 0x0\n" \
  "s_add_u32 s54, s54, 16\n" \
  "s_addc_u32 s55, s55, 0\n" \
  "s_waitcnt lgkmcnt(0)\n" \
  "v_and_b32 v245, 128, v244\n" \
  "v_lshrrev_b32 v245, 4, v245\n" \
  "v_add_u32 v245, v245, v251\n" \
  "s_mov_b32 s62, 0\n" \
  "s_mov_b32 s47, s80\n" \
  "s_lshl_b32 s60, s56, 7\n" \
  "s_add_u32 s60, s60, s58\n" \
  "s_add_u32 s60, s60, RH_PGRH\n" \
  "s_lshl_b32 s61, s56, 14\n" \
  "s_add_u32 s61, s61, RH_AM\n" \
  "s_mov_b64 s[68:69], s[64:65]\n" \
  "s_mov_b64 s[70:71], s[66:67]\n" \
  "s_sub_u32 s54, s54, 16\n" \
  "s_subb_u32 s55, s55, 0\n" \
  "s_mov_b32 s59, 0\n" \
  "La_slot:\n" \
  "v_mov_b64 v[232:233], 0\n" \
  "v_mov_b64 v[234:235], 0\n" \
  "v_mov_b64 v[236:237], 0\n" \
  "v_mov_b64 v[238:239], 0\n" \
  "s_and_b32 s76, s71, 0xff\n" \
  "s_cmp_ge_u32 s62, s76\n" \
  "s_cbranch_scc1 La_kv\n" \
  "s_mov_b32 s73, 0\n" \
  "La_poll:\n" \
  "buffer_load_dword v192, off, s[40:43], s47 sc1\n" \
  "s_waitcnt vmcnt(0)\n" \
  "v_readfirstlane_b32 s62, v192\n" \
  "s_cmp_ge_u32 s62, s76\n" \
  "s_cbranch_scc1 La_kv\n" \
  "s_add_u32 s73, s73, 1\n" \
  "s_cmp_lt_u32 s73, RH_CAP\n" \
  "s_cbranch_scc1 La_poll\n" \
  "v_mov_b32 v0, 1\n" \
  "buffer_store_dword v0, off, s[40:43], 0 offset:1280 sc1\n" \
  "s_endpgm\n" \
  "La_kv:\n" \
  "s_add_u32 s63, s68, 4096\n" \
  "La1_nd:\n" \
  "s_barrier\n" \
  "buffer_load_dwordx4 v[160:163], v253, s[40:43], s68 offen offset:0 sc1\n" \
  "buffer_load_dwordx4 v[176:179], v253, s[40:43], s68 offen offset:128 sc1\n" \
  "buffer_load_dwordx4 v[164:167], v253, s[40:43], s68 offen offset:2048 sc1\n" \
  "buffer_load_dwordx4 v[180:183], v253, s[40:43], s68 offen offset:2176 sc1\n" \
  "buffer_load_dwordx4 v[168:171], v253, s[40:43], s63 offen offset:0 sc1\n" \
  "buffer_load_dwordx4 v[184:187], v253, s[40:43], s63 offen offset:128 sc1\n" \
  "buffer_load_dwordx4 v[172:175], v253, s[40:43], s63 offen offset:2048 sc1\n" \
  "buffer_load_dwordx4 v[188:191], v253, s[40:43], s63 offen offset:2176 sc1\n" \
  "buffer_load_dwordx4 v[240:243], v248, s[40:43], s68 offen sc1\n" \
  "s_cmp_eq_u32 s59, 0\n" \
  "s_cbranch_scc1 La_ns\n" \
  "v_mov_b64 v[200:201], -1\n" \
  "buffer_store_dwordx2 v[200:201], v249, s[40:43], s66 offen sc1\n" \
  "buffer_store_dwordx2 v[204:205], v250, s[40:43], s64 offen sc1\n" \
  "buffer_store_dwordx4 v[208:211], v244, s[40:43], s77 offen offset:0 sc1\n" \
  "buffer_store_dwordx4 v[212:215], v244, s[40:43], s77 offen offset:1024 sc1\n" \
  "buffer_store_dwordx4 v[216:219], v244, s[40:43], s77 offen offset:2048 sc1\n" \
  "buffer_store_dwordx4 v[220:223], v244, s[40:43], s77 offen offset:3072 sc1\n" \
  "La_ns:\n" \
  "s_load_dwordx8 s[64:71], s[54:55], 0x0\n" \
  "s_add_u32 s54, s54, 16\n" \
  "s_addc_u32 s55, s55, 0\n" \
  "s_waitcnt lgkmcnt(0)\n" \
  "ds_read_b128 v[192:195], v244 offset:0\n" \
  "ds_read_b128 v[196:199], v244 offset:1024\n" \
  "ds_read_b128 v[200:203], v244 offset:2048\n" \
  "s_and_b32 s47, s71, 0x1000\n" \
  "s_lshl2_add_u32 s47, s47, s80\n" \
  "s_and_b32 s50, s67, 0x1000\n" \
  "s_add_u32 s50, s50, s60\n" \
  "s_add_u32 s77, s64, s61\n" \
  "; MARK1\n" \
  "s_waitcnt lgkmcnt(2)\n" \
  "v_smfmac_f32_16x16x64_bf16 v[232:235], v[192:195], v[0:7], v246\n" \
  "ds_read_b128 v[204:207], v244 offset:3072\n" \
  "v_smfmac_f32_16x16x64_bf16 v[236:239], v[192:195], v[80:87], v246\n" \
  "s_waitcnt lgkmcnt(2)\n" \
  "v_smfmac_f32_16x16x64_bf16 v[232:235], v[196:199], v[8:15], v246\n" \
  "ds_read_b128 v[208:211], v244 offset:4096\n" \
  "v_smfmac_f32_16x16x64_bf16 v[236:239], v[196:199], v[88:95], v246\n" \
  "s_waitcnt lgkmcnt(2)\n" \
  "v_smfmac_f32_16x16x64_bf16 v[232:235], v[200:203], v[16:23], v246\n" \
  "ds_read_b128 v[212:215], v244 offset:5120\n" \
  "v_smfmac_f32_16x16x64_bf16 v[236:239], v[200:203], v[96:103], v246\n" \
  "s_waitcnt lgkmcnt(2)\n" \
  "v_smfmac_f32_16x16x64_bf16 v[232:235], v[204:207], v[24:31], v246\n" \
  "ds_read_b128 v[216:219], v244 offset:6144\n" \
  "v_smfmac_f32_16x16x64_bf16 v[236:239], v[204:207], v[104:111], v246\n" \
  "s_waitcnt lgkmcnt(2)\n" \
  "v_smfmac_f32_16x16x64_bf16 v[232:235], v[208:211], v[32:39], v246\n" \
  "ds_read_b128 v[220:223], v244 offset:7168\n" \
  "v_smfmac_f32_16x16x64_bf16 v[236:239], v[208:211], v[112:119], v246\n" \
  "s_waitcnt lgkmcnt(2)\n" \
  "v_smfmac_f32_16x16x64_bf16 v[232:235], v[212:215], v[40:47], v246\n" \
  "ds_read_b128 v[224:227], v244 offset:8192\n" \
  "v_smfmac_f32_16x16x64_bf16 v[236:239], v[212:215], v[120:127], v246\n" \
  "s_waitcnt lgkmcnt(2)\n" \
  "v_smfmac_f32_16x16x64_bf16 v[232:235], v[216:219], v[48:55], v246\n" \
  "ds_read_b128 v[228:231], v244 offset:9216\n" \
  "v_smfmac_f32_16x16x64_bf16 v[236:239], v[216:219], v[128:135], v246\n" \
  "s_waitcnt lgkmcnt(2)\n" \
  "v_smfmac_f32_16x16x64_bf16 v[232:235], v[220:223], v[56:63], v246\n" \
  "s_waitcnt lgkmcnt(1)\n" \
  "v_smfmac_f32_16x16x64_bf16 v[232:235], v[224:227], v[64:71], v246\n" \
  "s_waitcnt lgkmcnt(0)\n" \
  "v_smfmac_f32_16x16x64_bf16 v[232:235], v[228:231], v[72:79], v246\n" \
  "v_smfmac_f32_16x16x64_bf16 v[236:239], v[220:223], v[136:143], v246\n" \
  "v_smfmac_f32_16x16x64_bf16 v[236:239], v[224:227], v[144:151], v246\n" \
  "v_smfmac_f32_16x16x64_bf16 v[236:239], v[228:231], v[152:159], v246\n" \
  "; MARK6\n" \
  "s_setprio 3\n" \
  "s_cmp_eq_u32 s59, 0\n" \
  "s_cbranch_scc1 La_w0\n" \
  "s_waitcnt vmcnt(6)\n" \
  "s_branch La_w1\n" \
  "La_w0:\n" \
  "s_waitcnt vmcnt(0)\n" \
  "s_nop 0\n" \
  "La_w1:\n" \
  "v_cvt_pk_bf16_f32 v232, v232, v233\n" \
  "v_cvt_pk_bf16_f32 v233, v234, v235\n" \
  "ds_write_b64 v247, v[232:233] offset:10240\n" \
  "v_cvt_pk_bf16_f32 v236, v236, v237\n" \
  "v_cvt_pk_bf16_f32 v237, v238, v239\n" \
  "ds_write_b64 v247, v[236:237] offset:14336\n" \
  "v_xor_b32 v228, 16, v254\n" \
  "v_lshrrev_b32 v238, 1, v244\n" \
  "s_mov_b64 exec, 1\n" \
  "buffer_load_dword v239, off, s[40:43], s47 sc1\n" \
  "s_mov_b64 exec, -1\n" \
  "s_waitcnt lgkmcnt(0)\n" \
  "s_barrier\n" \
  "ds_read_b64_tr_b16 v[192:193], v254 offset:10240\n" \
  "ds_read_b64_tr_b16 v[194:195], v228 offset:10496\n" \
  "ds_read_b64_tr_b16 v[196:197], v254 offset:11264\n" \
  "ds_read_b64_tr_b16 v[198:199], v228 offset:11520\n" \
  "ds_read_b64_tr_b16 v[200:201], v254 offset:12288\n" \
  "ds_read_b64_tr_b16 v[202:203], v228 offset:12544\n" \
  "ds_read_b64_tr_b16 v[204:205], v254 offset:13312\n" \
  "ds_read_b64_tr_b16 v[206:207], v228 offset:13568\n" \
  "ds_read_b64_tr_b16 v[208:209], v254 offset:14336\n" \
  "ds_read_b64_tr_b16 v[210:211], v228 offset:14592\n" \
  "s_waitcnt lgkmcnt(8)\n" \
  "v_mfma_f32_16x16x32_bf16 v[224:227], v[160:163], v[192:195], 0\n" \
  "ds_read_b64_tr_b16 v[212:213], v254 offset:15360\n" \
  "ds_read_b64_tr_b16 v[214:215], v228 offset:15616\n" \
  "s_waitcnt lgkmcnt(8)\n" \
  "v_mfma_f32_16x16x32_bf16 v[224:227], v[164:167], v[196:199], v[224:227]\n" \
  "ds_read_b64_tr_b16 v[216:217], v254 offset:16384\n" \
  "ds_read_b64_tr_b16 v[218:219], v228 offset:16640\n" \
  "s_waitcnt lgkmcnt(8)\n" \
  "v_mfma_f32_16x16x32_bf16 v[224:227], v[168:171], v[200:203], v[224:227]\n" \
  "ds_read_b64_tr_b16 v[220:221], v254 offset:17408\n" \
  "ds_read_b64_tr_b16 v[222:223], v228 offset:17664\n" \
  "s_waitcnt lgkmcnt(8)\n" \
  "v_mfma_f32_16x16x32_bf16 v[224:227], v[172:175], v[204:207], v[224:227]\n" \
  "s_waitcnt lgkmcnt(6)\n" \
  "v_mfma_f32_16x16x32_bf16 v[232:235], v[208:211], v[192:195], 0\n" \
  "s_waitcnt lgkmcnt(4)\n" \
  "v_mfma_f32_16x16x32_bf16 v[232:235], v[212:215], v[196:199], v[232:235]\n" \
  "s_waitcnt lgkmcnt(2)\n" \
  "v_mfma_f32_16x16x32_bf16 v[232:235], v[216:219], v[200:203], v[232:235]\n" \
  "s_waitcnt lgkmcnt(0)\n" \
  "v_mfma_f32_16x16x32_bf16 v[232:235], v[220:223], v[204:207], v[232:235]\n" \
  "v_mfma_f32_16x16x32_bf16 v[228:231], v[176:179], v[192:195], 0\n" \
  "v_add_f32_dpp v160, v224, v224 row_ror:8 row_mask:0xf bank_mask:0x3\n" \
  "v_add_f32_dpp v161, v225, v225 row_ror:8 row_mask:0xf bank_mask:0x3\n" \
  "v_mfma_f32_16x16x32_bf16 v[228:231], v[180:183], v[196:199], v[228:231]\n" \
  "v_add_f32_dpp v162, v226, v226 row_ror:8 row_mask:0xf bank_mask:0x3\n" \
  "v_add_f32_dpp v163, v227, v227 row_ror:8 row_mask:0xf bank_mask:0x3\n" \
  "v_mfma_f32_16x16x32_bf16 v[228:231], v[184:187], v[200:203], v[228:231]\n" \
  "s_nop 0\n" \
  "v_permlane32_swap_b32 v232, v233\n" \
  "v_mfma_f32_16x16x32_bf16 v[228:231], v[188:191], v[204:207], v[228:231]\n" \
  "; MARK7\n" \
  "v_permlane32_swap_b32 v234, v235\n" \
  "v_add_f32 v232, v232, v233\n" \
  "v_add_f32 v234, v234, v235\n" \
  "s_nop 0\n" \
  "v_add_f32_dpp v164, v232, v232 row_ror:8 row_mask:0xf bank_mask:0x3\n" \
  "v_add_f32_dpp v164, v234, v234 row_ror:8 row_mask:0xf bank_mask:0xc\n" \
  "s_nop 1\n" \
  "v_add_f32_dpp v160, v228, v228 row_ror:8 row_mask:0xf bank_mask:0xc\n" \
  "v_add_f32_dpp v161, v229, v229 row_ror:8 row_mask:0xf bank_mask:0xc\n" \
  "v_add_f32_dpp v162, v230, v230 row_ror:8 row_mask:0xf bank_mask:0xc\n" \
  "v_add_f32_dpp v163, v231, v231 row_ror:8 row_mask:0xf bank_mask:0xc\n" \
  "v_max3_f32 v165, v164, v160, v161\n" \
  "v_max3_f32 v165, v165, v162, v163\n" \
  "s_nop 1\n" \
  "v_max_f32_dpp v165, v165, v165 row_ror:8 row_mask:0xf bank_mask:0xf\n" \
  "v_mov_b32 v166, v165\n" \
  "s_nop 1\n" \
  "v_permlane16_swap_b32 v165, v166\n" \
  "v_max_f32 v165, v165, v166\n" \
  "v_mov_b32 v166, v165\n" \
  "s_nop 1\n" \
  "v_permlane32_swap_b32 v165, v166\n" \
  "v_max_f32 v165, v165, v166\n" \
  "v_mul_f32 v165, 0xbe0293ee, v165\n" \
  "v_fmamk_f32 v160, v160, 0x3e0293ee, v165\n" \
  "v_fmamk_f32 v161, v161, 0x3e0293ee, v165\n" \
  "v_fmamk_f32 v162, v162, 0x3e0293ee, v165\n" \
  "v_fmamk_f32 v163, v163, 0x3e0293ee, v165\n" \
  "v_fmamk_f32 v164, v164, 0x3e0293ee, v165\n" \
  "v_exp_f32 v160, v160\n" \
  "v_exp_f32 v161, v161\n" \
  "v_exp_f32 v162, v162\n" \
  "v_exp_f32 v163, v163\n" \
  "v_exp_f32 v164, v164\n" \
  "v_add_f32 v165, v160, v161\n" \
  "v_add_f32 v166, v162, v163\n" \
  "v_add_f32 v165, v165, v164\n" \
  "v_add_f32 v165, v165, v166\n" \
  "s_nop 1\n" \
  "v_add_f32_dpp v165, v165, v165 row_ror:8 row_mask:0xf bank_mask:0xf\n" \
  "v_mov_b32 v166, v165\n" \
  "s_nop 1\n" \
  "v_permlane16_swap_b32 v165, v166\n" \
  "v_add_f32 v165, v165, v166\n" \
  "v_mov_b32 v166, v165\n" \
  "s_nop 1\n" \
  "v_permlane32_swap_b32 v165, v166\n" \
  "v_add_f32 v165, v165, v166\n" \
  "v_rcp_f32 v165, v165\n" \
  "v_mov_b64 v[198:199], 0\n" \
  "v_pk_mul_f32 v[160:161], v[160:161], v[164:165] op_sel:[0,1] op_sel_hi:[1,1]\n" \
  "v_pk_mul_f32 v[162:163], v[162:163], v[164:165] op_sel:[0,1] op_sel_hi:[1,1]\n" \
  "v_mul_f32 v164, v164, v165\n" \
  "v_cvt_pk_bf16_f32 v160, v160, v161\n" \
  "v_cvt_pk_bf16_f32 v161, v162, v163\n" \
  "v_cvt_pk_bf16_f32 v164, v164, v164\n" \
  "ds_write_b64 v245, v[160:161] offset:18432\n" \
  "ds_write_b16 v255, v164\n" \
  "s_waitcnt lgkmcnt(0)\n" \
  "s_barrier\n" \
  "ds_read_b128 v[224:227], v251 offset:18432\n" \
  "ds_read_b64 v[196:197], v252\n" \
  "ds_read_b64 v[204:205], v238 offset:19200\n" \
  "s_waitcnt lgkmcnt(2)\n" \
  "v_mfma_f32_16x16x32_bf16 v[228:231], v[240:243], v[224:227], 0\n" \
  "s_waitcnt lgkmcnt(0)\n" \
  "v_mfma_f32_16x16x32_bf16 v[228:231], v[204:207], v[196:199], v[228:231]\n" \
  "s_nop 7\n" \
  "v_cvt_pk_bf16_f32 v228, v228, v229\n" \
  "v_cvt_pk_bf16_f32 v229, v230, v231\n" \
  "v_min_u32 v228, -2, v228\n" \
  "buffer_store_dwordx2 v[228:229], v249, s[40:43], s65 offen sc1\n" \
  "s_setprio 0\n" \
  "s_waitcnt vmcnt(1)\n" \
  "v_readfirstlane_b32 s62, v239\n" \
  "s_cmp_eq_u32 s59, 0\n" \
  "s_cbranch_scc1 La_nf\n" \
  "v_mov_b32 v192, s59\n" \
  "s_mov_b64 exec, 1\n" \
  "buffer_store_dword v192, off, s[40:43], s52 sc1\n" \
  "s_mov_b64 exec, -1\n" \
  "La_nf:\n" \
  "s_mov_b32 s52, s50\n" \
  "s_mov_b32 s59, s76\n" \
  "s_bitcmp1_b32 s67, 16\n" \
  "s_cbranch_scc0 La_df\n" \
  "s_nop 7\n" \
  "v_mov_b64 v[200:201], -1\n" \
  "buffer_store_dwordx2 v[200:201], v249, s[40:43], s66 offen sc1\n" \
  "buffer_store_dwordx2 v[204:205], v250, s[40:43], s64 offen sc1\n" \
  "buffer_store_dwordx4 v[208:211], v244, s[40:43], s77 offen offset:0 sc1\n" \
  "buffer_store_dwordx4 v[212:215], v244, s[40:43], s77 offen offset:1024 sc1\n" \
  "buffer_store_dwordx4 v[216:219], v244, s[40:43], s77 offen offset:2048 sc1\n" \
  "buffer_store_dwordx4 v[220:223], v244, s[40:43], s77 offen offset:3072 sc1\n" \
  "v_mov_b32 v192, s76\n" \
  "s_waitcnt vmcnt(0)\n" \
  "s_barrier\n" \
  "s_mov_b64 exec, 1\n" \
  "buffer_store_dword v192, off, s[40:43], s50 sc1\n" \
  "s_mov_b64 exec, -1\n" \
  "s_mov_b32 s59, 0\n" \
  "La_df:\n" \
  "s_bitcmp1_b32 s67, 17\n" \
  "s_cbranch_scc0 La_slot\n" \
  "s_endpgm\n"
#define RW_ASM \
  "s_add_u32 m0, s78, 0\n" \
  "s_add_u32 s74, s79, 0\n" \
  "buffer_load_dwordx4 v240, s[44:47], s74 offen lds\n" \
  "s_add_u32 m0, s78, 1024\n" \
  "s_add_u32 s74, s79, 20480\n" \
  "buffer_load_dwordx4 v240, s[44:47], s74 offen lds\n" \
  "s_add_u32 m0, s78, 2048\n" \
  "s_add_u32 s74, s79, 40960\n" \
  "buffer_load_dwordx4 v240, s[44:47], s74 offen lds\n" \
  "s_add_u32 m0, s78, 3072\n" \
  "s_add_u32 s74, s79, 61440\n" \
  "buffer_load_dwordx4 v240, s[44:47], s74 offen lds\n" \
  "s_add_u32 m0, s78, 4096\n" \
  "s_add_u32 s74, s79, 81920\n" \
  "buffer_load_dwordx4 v240, s[44:47], s74 offen lds\n" \
  "s_add_u32 m0, s78, 5120\n" \
  "s_add_u32 s74, s79, 102400\n" \
  "buffer_load_dwordx4 v240, s[44:47], s74 offen lds\n" \
  "s_add_u32 m0, s78, 6144\n" \
  "s_add_u32 s74, s79, 122880\n" \
  "buffer_load_dwordx4 v240, s[44:47], s74 offen lds\n" \
  "s_add_u32 m0, s78, 7168\n" \
  "s_add_u32 s74, s79, 143360\n" \
  "buffer_load_dwordx4 v240, s[44:47], s74 offen lds\n" \
  "s_add_u32 m0, s78, 8192\n" \
  "s_add_u32 s74, s79, 163840\n" \
  "buffer_load_dwordx4 v240, s[44:47], s74 offen lds\n" \
  "s_add_u32 m0, s78, 9216\n" \
  "s_add_u32 s74, s79, 184320\n" \
  "buffer_load_dwordx4 v240, s[44:47], s74 offen lds\n" \
  "s_add_u32 m0, s78, 10240\n" \
  "s_add_u32 s74, s79, 204800\n" \
  "buffer_load_dwordx4 v240, s[44:47], s74 offen lds\n" \
  "s_add_u32 m0, s78, 11264\n" \
  "s_add_u32 s74, s79, 225280\n" \
  "buffer_load_dwordx4 v240, s[44:47], s74 offen lds\n" \
  "s_add_u32 m0, s78, 12288\n" \
  "s_add_u32 s74, s79, 245760\n" \
  "buffer_load_dwordx4 v240, s[44:47], s74 offen lds\n" \
  "s_add_u32 m0, s78, 13312\n" \
  "s_add_u32 s74, s79, 266240\n" \
  "buffer_load_dwordx4 v240, s[44:47], s74 offen lds\n" \
  "s_add_u32 m0, s78, 14336\n" \
  "s_add_u32 s74, s79, 286720\n" \
  "buffer_load_dwordx4 v240, s[44:47], s74 offen lds\n" \
  "s_add_u32 m0, s78, 15360\n" \
  "s_add_u32 s74, s79, 307200\n" \
  "buffer_load_dwordx4 v240, s[44:47], s74 offen lds\n" \
  "s_waitcnt vmcnt(0)\n" \
  "ds_read_b64_tr_b16 v[0:1], v241 offset:0\n" \
  "ds_read_b64_tr_b16 v[2:3], v241 offset:128\n" \
  "ds_read_b64_tr_b16 v[4:5], v241 offset:2048\n" \
  "ds_read_b64_tr_b16 v[6:7], v241 offset:2176\n" \
  "ds_read_b64_tr_b16 v[8:9], v241 offset:32\n" \
  "ds_read_b64_tr_b16 v[10:11], v241 offset:160\n" \
  "ds_read_b64_tr_b16 v[12:13], v241 offset:2080\n" \
  "ds_read_b64_tr_b16 v[14:15], v241 offset:2208\n" \
  "s_waitcnt lgkmcnt(0)\n" \
  "ds_read_b64_tr_b16 v[16:17], v241 offset:4096\n" \
  "ds_read_b64_tr_b16 v[18:19], v241 offset:4224\n" \
  "ds_read_b64_tr_b16 v[20:21], v241 offset:6144\n" \
  "ds_read_b64_tr_b16 v[22:23], v241 offset:6272\n" \
  "ds_read_b64_tr_b16 v[24:25], v241 offset:4128\n" \
  "ds_read_b64_tr_b16 v[26:27], v241 offset:4256\n" \
  "ds_read_b64_tr_b16 v[28:29], v241 offset:6176\n" \
  "ds_read_b64_tr_b16 v[30:31], v241 offset:6304\n" \
  "s_waitcnt lgkmcnt(0)\n" \
  "ds_read_b64_tr_b16 v[32:33], v241 offset:8192\n" \
  "ds_read_b64_tr_b16 v[34:35], v241 offset:8320\n" \
  "ds_read_b64_tr_b16 v[36:37], v241 offset:10240\n" \
  "ds_read_b64_tr_b16 v[38:39], v241 offset:10368\n" \
  "ds_read_b64_tr_b16 v[40:41], v241 offset:8224\n" \
  "ds_read_b64_tr_b16 v[42:43], v241 offset:8352\n" \
  "ds_read_b64_tr_b16 v[44:45], v241 offset:10272\n" \
  "ds_read_b64_tr_b16 v[46:47], v241 offset:10400\n" \
  "s_waitcnt lgkmcnt(0)\n" \
  "ds_read_b64_tr_b16 v[48:49], v241 offset:12288\n" \
  "ds_read_b64_tr_b16 v[50:51], v241 offset:12416\n" \
  "ds_read_b64_tr_b16 v[52:53], v241 offset:14336\n" \
  "ds_read_b64_tr_b16 v[54:55], v241 offset:14464\n" \
  "ds_read_b64_tr_b16 v[56:57], v241 offset:12320\n" \
  "ds_read_b64_tr_b16 v[58:59], v241 offset:12448\n" \
  "ds_read_b64_tr_b16 v[60:61], v241 offset:14368\n" \
  "ds_read_b64_tr_b16 v[62:63], v241 offset:14496\n" \
  "s_waitcnt lgkmcnt(0)\n" \
  "s_add_u32 m0, s78, 0\n" \
  "s_add_u32 s74, s79, 327680\n" \
  "buffer_load_dwordx4 v240, s[44:47], s74 offen lds\n" \
  "s_add_u32 m0, s78, 1024\n" \
  "s_add_u32 s74, s79, 348160\n" \
  "buffer_load_dwordx4 v240, s[44:47], s74 offen lds\n" \
  "s_add_u32 m0, s78, 2048\n" \
  "s_add_u32 s74, s79, 368640\n" \
  "buffer_load_dwordx4 v240, s[44:47], s74 offen lds\n" \
  "s_add_u32 m0, s78, 3072\n" \
  "s_add_u32 s74, s79, 389120\n" \
  "buffer_load_dwordx4 v240, s[44:47], s74 offen lds\n" \
  "s_add_u32 m0, s78, 4096\n" \
  "s_add_u32 s74, s79, 409600\n" \
  "buffer_load_dwordx4 v240, s[44:47], s74 offen lds\n" \
  "s_add_u32 m0, s78, 5120\n" \
  "s_add_u32 s74, s79, 430080\n" \
  "buffer_load_dwordx4 v240, s[44:47], s74 offen lds\n" \
  "s_add_u32 m0, s78, 6144\n" \
  "s_add_u32 s74, s79, 450560\n" \
  "buffer_load_dwordx4 v240, s[44:47], s74 offen lds\n" \
  "s_add_u32 m0, s78, 7168\n" \
  "s_add_u32 s74, s79, 471040\n" \
  "buffer_load_dwordx4 v240, s[44:47], s74 offen lds\n" \
  "s_add_u32 m0, s78, 8192\n" \
  "s_add_u32 s74, s79, 491520\n" \
  "buffer_load_dwordx4 v240, s[44:47], s74 offen lds\n" \
  "s_add_u32 m0, s78, 9216\n" \
  "s_add_u32 s74, s79, 512000\n" \
  "buffer_load_dwordx4 v240, s[44:47], s74 offen lds\n" \
  "s_add_u32 m0, s78, 10240\n" \
  "s_add_u32 s74, s79, 532480\n" \
  "buffer_load_dwordx4 v240, s[44:47], s74 offen lds\n" \
  "s_add_u32 m0, s78, 11264\n" \
  "s_add_u32 s74, s79, 552960\n" \
  "buffer_load_dwordx4 v240, s[44:47], s74 offen lds\n" \
  "s_add_u32 m0, s78, 12288\n" \
  "s_add_u32 s74, s79, 573440\n" \
  "buffer_load_dwordx4 v240, s[44:47], s74 offen lds\n" \
  "s_add_u32 m0, s78, 13312\n" \
  "s_add_u32 s74, s79, 593920\n" \
  "buffer_load_dwordx4 v240, s[44:47], s74 offen lds\n" \
  "s_add_u32 m0, s78, 14336\n" \
  "s_add_u32 s74, s79, 614400\n" \
  "buffer_load_dwordx4 v240, s[44:47], s74 offen lds\n" \
  "s_add_u32 m0, s78, 15360\n" \
  "s_add_u32 s74, s79, 634880\n" \
  "buffer_load_dwordx4 v240, s[44:47], s74 offen lds\n" \
  "s_waitcnt vmcnt(0)\n" \
  "ds_read_b64_tr_b16 v[64:65], v241 offset:0\n" \
  "ds_read_b64_tr_b16 v[66:67], v241 offset:128\n" \
  "ds_read_b64_tr_b16 v[68:69], v241 offset:2048\n" \
  "ds_read_b64_tr_b16 v[70:71], v241 offset:2176\n" \
  "ds_read_b64_tr_b16 v[72:73], v241 offset:32\n" \
  "ds_read_b64_tr_b16 v[74:75], v241 offset:160\n" \
  "ds_read_b64_tr_b16 v[76:77], v241 offset:2080\n" \
  "ds_read_b64_tr_b16 v[78:79], v241 offset:2208\n" \
  "s_waitcnt lgkmcnt(0)\n" \
  "ds_read_b64_tr_b16 v[80:81], v241 offset:4096\n" \
  "ds_read_b64_tr_b16 v[82:83], v241 offset:4224\n" \
  "ds_read_b64_tr_b16 v[84:85], v241 offset:6144\n" \
  "ds_read_b64_tr_b16 v[86:87], v241 offset:6272\n" \
  "ds_read_b64_tr_b16 v[88:89], v241 offset:4128\n" \
  "ds_read_b64_tr_b16 v[90:91], v241 offset:4256\n" \
  "ds_read_b64_tr_b16 v[92:93], v241 offset:6176\n" \
  "ds_read_b64_tr_b16 v[94:95], v241 offset:6304\n" \
  "s_waitcnt lgkmcnt(0)\n" \
  "ds_read_b64_tr_b16 v[96:97], v241 offset:8192\n" \
  "ds_read_b64_tr_b16 v[98:99], v241 offset:8320\n" \
  "ds_read_b64_tr_b16 v[100:101], v241 offset:10240\n" \
  "ds_read_b64_tr_b16 v[102:103], v241 offset:10368\n" \
  "ds_read_b64_tr_b16 v[104:105], v241 offset:8224\n" \
  "ds_read_b64_tr_b16 v[106:107], v241 offset:8352\n" \
  "ds_read_b64_tr_b16 v[108:109], v241 offset:10272\n" \
  "ds_read_b64_tr_b16 v[110:111], v241 offset:10400\n" \
  "s_waitcnt lgkmcnt(0)\n" \
  "ds_read_b64_tr_b16 v[112:113], v241 offset:12288\n" \
  "ds_read_b64_tr_b16 v[114:115], v241 offset:12416\n" \
  "ds_read_b64_tr_b16 v[116:117], v241 offset:14336\n" \
  "ds_read_b64_tr_b16 v[118:119], v241 offset:14464\n" \
  "ds_read_b64_tr_b16 v[120:121], v241 offset:12320\n" \
  "ds_read_b64_tr_b16 v[122:123], v241 offset:12448\n" \
  "ds_read_b64_tr_b16 v[124:125], v241 offset:14368\n" \
  "ds_read_b64_tr_b16 v[126:127], v241 offset:14496\n" \
  "s_waitcnt lgkmcnt(0)\n" \
  "s_barrier\n" \
  "v_lshrrev_b32 v250, 1, v244\n" \
  "s_mov_b32 s60, 0\n" \
  "s_mov_b32 s61, 0\n" \
  "s_mov_b32 s65, 0\n" \
  "s_lshl_b32 s70, s60, 12\n" \
  "s_add_u32 s70, s70, s58\n" \
  "s_add_u32 s70, s70, RH_HINT\n" \
  "v_mov_b32 v176, 0\n" \
  "Lw_slot:\n" \
  "s_and_b32 s75, s61, 3\n" \
  "s_lshl2_add_u32 s75, s60, s75\n" \
  "s_mul_i32 s64, s75, 163840\n" \
  "s_add_u32 s64, s64, s80\n" \
  "s_add_u32 s76, s61, 1\n" \
  "s_mov_b32 s73, 0\n" \
  "s_branch Lw_hc\n" \
  "Lw_hp:\n" \
  "buffer_load_dword v176, v247, s[40:43], s70 offen sc1\n" \
  "s_waitcnt vmcnt(0)\n" \
  "Lw_hc:\n" \
  "v_and_b32 v178, 0xff, v176\n" \
  "v_cmp_gt_u32 vcc, s76, v178\n" \
  "s_cbranch_vccz Lw_tw\n" \
  "s_add_u32 s73, s73, 1\n" \
  "s_cmp_lt_u32 s73, RH_CAP\n" \
  "s_cbranch_scc1 Lw_hp\n" \
  "v_mov_b32 v0, 1\n" \
  "buffer_store_dword v0, off, s[40:43], 0 offset:1280 sc1\n" \
  "s_endpgm\n" \
  "Lw_tw:\n" \
  "s_and_b32 s75, s76, 3\n" \
  "s_lshl2_add_u32 s75, s60, s75\n" \
  "s_mul_i32 s68, s75, 163840\n" \
  "s_add_u32 s68, s68, s62\n" \
  "s_xor_b32 s75, s75, 2\n" \
  "s_mul_i32 s69, s75, 163840\n" \
  "s_add_u32 s69, s69, s62\n" \
  "s_cmp_eq_u32 s60, 0\n" \
  "s_cselect_b32 s74, 0-RH_LAG, RH_LAG+1\n" \
  "s_add_u32 s74, s74, s61\n" \
  "s_add_u32 s61, s61, 1\n" \
  "s_xor_b32 s75, s60, 1\n" \
  "s_cmp_lt_u32 s74, 63\n" \
  "s_cselect_b32 s61, s74, s61\n" \
  "s_cselect_b32 s60, s75, s60\n" \
  "s_lshl_b32 s70, s60, 12\n" \
  "s_add_u32 s70, s70, s58\n" \
  "s_add_u32 s70, s70, RH_HINT\n" \
  "Lw_tl:\n" \
  "s_memrealtime s[44:45]\n" \
  "s_waitcnt lgkmcnt(0)\n" \
  "s_lshl_b32 s74, s44, 8\n" \
  "v_subrev_u32 v178, s74, v176\n" \
  "v_cmp_lt_i32 vcc, 0xff, v178\n" \
  "s_cbranch_vccz Lw_l0\n" \
  "s_add_u32 s73, s73, 1\n" \
  "s_cmp_lt_u32 s73, RH_CAP\n" \
  "s_cbranch_scc1 Lw_tl\n" \
  "v_mov_b32 v0, 1\n" \
  "buffer_store_dword v0, off, s[40:43], 0 offset:1280 sc1\n" \
  "s_endpgm\n" \
  "Lw_l0:\n" \
  "s_mov_b32 s73, 0\n" \
  "buffer_load_dwordx4 v[128:131], v244, s[40:43], s64 offen offset:0 sc1\n" \
  "v_mov_b32 v144, 0\n" \
  "v_mov_b32 v145, 0\n" \
  "v_mov_b32 v146, 0\n" \
  "v_mov_b32 v147, 0\n" \
  "v_mov_b32 v148, 0\n" \
  "v_mov_b32 v149, 0\n" \
  "s_nop 7\n" \
  "buffer_load_dwordx4 v[132:135], v244, s[40:43], s64 offen offset:1024 sc1\n" \
  "v_mov_b32 v150, 0\n" \
  "v_mov_b32 v151, 0\n" \
  "v_mov_b32 v152, 0\n" \
  "v_mov_b32 v153, 0\n" \
  "v_mov_b32 v154, 0\n" \
  "v_mov_b32 v155, 0\n" \
  "s_nop 7\n" \
  "buffer_load_dwordx4 v[136:139], v244, s[40:43], s64 offen offset:2048 sc1\n" \
  "v_mov_b32 v156, 0\n" \
  "v_mov_b32 v157, 0\n" \
  "v_mov_b32 v158, 0\n" \
  "v_mov_b32 v159, 0\n" \
  "s_nop 7\n" \
  "buffer_load_dwordx4 v[140:143], v244, s[40:43], s64 offen offset:3072 sc1\n" \
  "s_waitcnt vmcnt(3)\n" \
  "Lw_c0:\n" \
  "v_max_u32 v178, v128, v130\n" \
  "v_cmp_eq_u32 vcc, -1, v178\n" \
  "s_cbranch_vccnz Lw_r0\n" \
  "v_smfmac_f32_16x16x64_bf16 v[144:147], v[128:131], v[0:7], v246\n" \
  "v_smfmac_f32_16x16x64_bf16 v[152:155], v[128:131], v[8:15], v246\n" \
  "v_smfmac_f32_16x16x64_bf16 v[148:151], v[128:131], v[16:23], v246\n" \
  "v_smfmac_f32_16x16x64_bf16 v[156:159], v[128:131], v[24:31], v246\n" \
  "s_waitcnt vmcnt(2)\n" \
  "Lw_c1:\n" \
  "v_max_u32 v178, v132, v134\n" \
  "v_cmp_eq_u32 vcc, -1, v178\n" \
  "s_cbranch_vccnz Lw_r1\n" \
  "v_smfmac_f32_16x16x64_bf16 v[144:147], v[132:135], v[32:39], v246\n" \
  "v_smfmac_f32_16x16x64_bf16 v[152:155], v[132:135], v[40:47], v246\n" \
  "v_smfmac_f32_16x16x64_bf16 v[148:151], v[132:135], v[48:55], v246\n" \
  "v_smfmac_f32_16x16x64_bf16 v[156:159], v[132:135], v[56:63], v246\n" \
  "s_waitcnt vmcnt(1)\n" \
  "Lw_c2:\n" \
  "v_max_u32 v178, v136, v138\n" \
  "v_cmp_eq_u32 vcc, -1, v178\n" \
  "s_cbranch_vccnz Lw_r2\n" \
  "v_smfmac_f32_16x16x64_bf16 v[144:147], v[136:139], v[64:71], v246\n" \
  "v_smfmac_f32_16x16x64_bf16 v[152:155], v[136:139], v[72:79], v246\n" \
  "v_smfmac_f32_16x16x64_bf16 v[148:151], v[136:139], v[80:87], v246\n" \
  "v_smfmac_f32_16x16x64_bf16 v[156:159], v[136:139], v[88:95], v246\n" \
  "s_waitcnt vmcnt(0)\n" \
  "Lw_c3:\n" \
  "v_max_u32 v178, v140, v142\n" \
  "v_cmp_eq_u32 vcc, -1, v178\n" \
  "s_cbranch_vccnz Lw_r3\n" \
  "v_smfmac_f32_16x16x64_bf16 v[144:147], v[140:143], v[96:103], v246\n" \
  "v_smfmac_f32_16x16x64_bf16 v[152:155], v[140:143], v[104:111], v246\n" \
  "v_smfmac_f32_16x16x64_bf16 v[148:151], v[140:143], v[112:119], v246\n" \
  "v_smfmac_f32_16x16x64_bf16 v[156:159], v[140:143], v[120:127], v246\n" \
  "Lw_ok:\n" \
  "buffer_load_dword v176, v247, s[40:43], s70 offen sc1\n" \
  "v_add_u32 v178, s65, v248\n" \
  "s_nop 5\n" \
  "v_permlane32_swap_b32 v148, v152\n" \
  "v_permlane32_swap_b32 v149, v153\n" \
  "v_permlane32_swap_b32 v150, v154\n" \
  "v_permlane32_swap_b32 v151, v155\n" \
  "v_add_f32_dpp v144, v152, v144 quad_perm:[0,1,2,3] row_mask:0x3 bank_mask:0xf\n" \
  "v_add_f32_dpp v145, v153, v145 quad_perm:[0,1,2,3] row_mask:0x3 bank_mask:0xf\n" \
  "v_add_f32_dpp v146, v154, v146 quad_perm:[0,1,2,3] row_mask:0x3 bank_mask:0xf\n" \
  "v_add_f32_dpp v147, v155, v147 quad_perm:[0,1,2,3] row_mask:0x3 bank_mask:0xf\n" \
  "v_add_f32_dpp v144, v148, v156 quad_perm:[0,1,2,3] row_mask:0xc bank_mask:0xf\n" \
  "v_add_f32_dpp v145, v149, v157 quad_perm:[0,1,2,3] row_mask:0xc bank_mask:0xf\n" \
  "v_add_f32_dpp v146, v150, v158 quad_perm:[0,1,2,3] row_mask:0xc bank_mask:0xf\n" \
  "v_add_f32_dpp v147, v151, v159 quad_perm:[0,1,2,3] row_mask:0xc bank_mask:0xf\n" \
  "s_nop 0\n" \
  "v_permlane16_swap_b32 v144, v146\n" \
  "v_permlane16_swap_b32 v145, v147\n" \
  "v_add_f32 v144, v144, v146\n" \
  "v_add_f32 v145, v145, v147\n" \
  "ds_write2_b32 v178, v144, v145 offset0:0 offset1:8\n" \
  "v_add_u32 v178, s65, v250\n" \
  "s_xor_b32 s65, s65, 4096\n" \
  "s_cmp_lg_u32 s57, 0\n" \
  "s_waitcnt lgkmcnt(0)\n" \
  "s_barrier\n" \
  "s_cbranch_scc1 Lw_np\n" \
  "ds_read_b64 v[160:161], v178 offset:0\n" \
  "ds_read_b64 v[162:163], v178 offset:512\n" \
  "ds_read_b64 v[164:165], v178 offset:1024\n" \
  "ds_read_b64 v[166:167], v178 offset:1536\n" \
  "ds_read_b64 v[168:169], v178 offset:2048\n" \
  "ds_read_b64 v[170:171], v178 offset:2560\n" \
  "ds_read_b64 v[172:173], v178 offset:3072\n" \
  "ds_read_b64 v[174:175], v178 offset:3584\n" \
  "buffer_load_dword v177, v247, s[40:43], s70 offen sc1\n" \
  "s_waitcnt lgkmcnt(6)\n" \
  "v_add_f32 v160, v160, v162\n" \
  "v_add_f32 v161, v161, v163\n" \
  "s_waitcnt lgkmcnt(4)\n" \
  "v_add_f32 v164, v164, v166\n" \
  "v_add_f32 v165, v165, v167\n" \
  "s_waitcnt lgkmcnt(2)\n" \
  "v_add_f32 v168, v168, v170\n" \
  "v_add_f32 v169, v169, v171\n" \
  "v_add_f32 v160, v160, v164\n" \
  "v_add_f32 v161, v161, v165\n" \
  "s_waitcnt lgkmcnt(0)\n" \
  "v_add_f32 v172, v172, v174\n" \
  "v_add_f32 v173, v173, v175\n" \
  "v_add_f32 v160, v160, v168\n" \
  "v_add_f32 v161, v161, v169\n" \
  "v_add_f32 v160, v160, v172\n" \
  "v_add_f32 v161, v161, v173\n" \
  "v_cvt_pk_bf16_f32 v160, v160, v161\n" \
  "v_min_u32 v160, -2, v160\n" \
  "buffer_store_dword v160, v249, s[40:43], s68 offen sc1\n" \
  "v_mov_b32 v161, -1\n" \
  "buffer_store_dword v161, v249, s[40:43], s69 offen sc1\n" \
  "s_waitcnt vmcnt(3)\n" \
  "v_and_b32 v178, 0xff, v176\n" \
  "v_cmp_ge_u32 vcc, s61, v178\n" \
  "s_cbranch_vccz Lw_h1\n" \
  "s_waitcnt vmcnt(2)\n" \
  "v_mov_b32 v176, v177\n" \
  "Lw_h1:\n" \
  "s_branch Lw_nq\n" \
  "Lw_np:\n" \
  "s_waitcnt vmcnt(0)\n" \
  "Lw_nq:\n" \
  "s_cmp_lt_u32 s61, 63\n" \
  "s_cbranch_scc1 Lw_slot\n" \
  "s_endpgm\n" \
  "Lw_r0:\n" \
  "buffer_load_dwordx4 v[128:131], v244, s[40:43], s64 offen offset:0 sc1\n" \
  "s_waitcnt vmcnt(0)\n" \
  "s_add_u32 s73, s73, 1\n" \
  "s_cmp_lt_u32 s73, RH_CAP\n" \
  "s_cbranch_scc1 Lw_c0\n" \
  "v_mov_b32 v0, 1\n" \
  "buffer_store_dword v0, off, s[40:43], 0 offset:1280 sc1\n" \
  "s_endpgm\n" \
  "Lw_r1:\n" \
  "buffer_load_dwordx4 v[132:135], v244, s[40:43], s64 offen offset:1024 sc1\n" \
  "s_waitcnt vmcnt(0)\n" \
  "s_add_u32 s73, s73, 1\n" \
  "s_cmp_lt_u32 s73, RH_CAP\n" \
  "s_cbranch_scc1 Lw_c1\n" \
  "v_mov_b32 v0, 1\n" \
  "buffer_store_dword v0, off, s[40:43], 0 offset:1280 sc1\n" \
  "s_endpgm\n" \
  "Lw_r2:\n" \
  "buffer_load_dwordx4 v[136:139], v244, s[40:43], s64 offen offset:2048 sc1\n" \
  "s_waitcnt vmcnt(0)\n" \
  "s_add_u32 s73, s73, 1\n" \
  "s_cmp_lt_u32 s73, RH_CAP\n" \
  "s_cbranch_scc1 Lw_c2\n" \
  "v_mov_b32 v0, 1\n" \
  "buffer_store_dword v0, off, s[40:43], 0 offset:1280 sc1\n" \
  "s_endpgm\n" \
  "Lw_r3:\n" \
  "buffer_load_dwordx4 v[140:143], v244, s[40:43], s64 offen offset:3072 sc1\n" \
  "s_waitcnt vmcnt(0)\n" \
  "s_add_u32 s73, s73, 1\n" \
  "s_cmp_lt_u32 s73, RH_CAP\n" \
  "s_cbranch_scc1 Lw_c3\n" \
  "v_mov_b32 v0, 1\n" \
  "buffer_store_dword v0, off, s[40:43], 0 offset:1280 sc1\n" \
  "s_endpgm\n"

#define RH_SETS() asm volatile(".set RH_CAP, %c0\n.set RH_DT, %c1\n.set RH_LAG, %c2\n.set RH_HINT, %c3\n.set RH_PGRH, %c4\n.set RH_WSB, %c5\n.set RH_TAB, %c6\n.set RH_AM, %c7\n" \
  :: "i"(SPIN_CAP), "i"(HINT_DT), "i"(LAG), "i"(RB_HINT * BK), "i"(PA_RH), "i"(WS_END), "i"(CT_TAB), "i"(A_M))
template <bool AW>
__device__ __forceinline__ void rh_wave(const Ctx& c, int h) {
  const int wave = c.wave, lane = c.lane, g = lane >> 4, cc = lane & 15, q = (lane >> 2) & 3;
  const unsigned vl = lane * 16, vwa = RH_SL + wave * 2048 + lane * 16, sidx = (lane & 8) ? 0xEEEE : 0x4444;
  const unsigned img = (16 * wave + cc) * 32 + (g ^ (cc >> 2)) * 8;
  const unsigned vvx = A_V + h * BK + (wave >> 1) * 2048 + g * 512 + (16 * (wave & 1) + cc) * 16;
  const unsigned vo = cc < 8 ? c.ring + (h >> 3) * BK + (cc >> 2) * 65536 + (h & 7) * 1024 + (wave & 3) * 256 + (wave >> 2) * 128 + (g >> 1) * 64 + (cc & 3) * 16 + (g & 1) * 8 : OOB;
  const unsigned vvm = A_M + h * BK + 4096 + (wave >> 1) * 1024 + (g & 1) * 512 + (16 * (wave & 1) + cc) * 16 + (g >> 1) * 8;
  const unsigned prd = g * 128 + (cc & 7) * 16, pmr = RH_PM + (g & 1) * 64 + (cc & 7) * 8;
  const unsigned vw = (lane >> 4) * 8192 + (((lane & 15) * 16) ^ ((g & 1) * 32 + (wave & 1) * 128));
  const unsigned tp = RH_SL + (16 * (g >> 1) + 8 * (q >> 1) + 4 * (g & 1) + (q & 1)) * 256 + (wave ^ ((q & 1) + 2 * (g & 1) + 4 * (q >> 1))) * 32 + (lane & 3) * 8;
  const unsigned vkx = AW ? A_K + h * BK + g * 512 + (16 * (cc >> 3) + (cc & 7)) * 16 : c.ring + (8 + (wave >> 2)) * BK + (wave & 3) * 2048 + (lane >> 5) * 512 + ((lane >> 2) & 1) * 256 + ((lane >> 3) & 3) * 64 + (lane & 3) * 16;
  const unsigned trq = (g >> 1) * 512 + (g & 1) * 128 + (cc ^ (g & 1)) * 8;
  const unsigned pmw = pmr + ((g >> 1) + 2 * (cc >> 3)) * 2;
  const unsigned dst = RH_SL + wave * 2048, wb = wave * 8 * 8192 + h * 256;
  const unsigned fn = c.ring + PA_QKV + h * 128;
  RH_SETS();
#define RH_PRO \
  "s_mov_b64 s[40:41], %0\n s_mov_b32 s42, RH_WSB\n s_mov_b32 s43, 0x20000\n" \
  "s_mov_b64 s[44:45], %1\n s_mov_b32 s46, 0x500000\n s_mov_b32 s47, 0x20000\n" \
  "s_mov_b64 s[48:49], %2\n s_mov_b32 s50, 0x500000\n s_mov_b32 s51, 0x20000\n" \
  "s_mov_b64 s[52:53], %3\n s_mov_b32 s54, 0x500000\n s_mov_b32 s55, 0x20000\n" \
  "s_and_b32 s41, s41, 0xffff\n s_and_b32 s45, s45, 0xffff\n s_and_b32 s49, s49, 0xffff\n s_and_b32 s53, s53, 0xffff\n" \
  "s_mov_b32 s56, %4\n s_mov_b32 s57, %5\n s_mov_b32 s58, %6\n s_mov_b32 s78, %7\n s_mov_b32 s79, %8\n s_mov_b32 s80, %9\n s_mov_b32 s81, %10\n" \
  "v_mov_b32 v244, %11\n v_mov_b32 v245, %12\n v_mov_b32 v246, %13\n v_mov_b32 v247, %14\n v_mov_b32 v248, %15\n v_mov_b32 v249, %16\n" \
  "v_mov_b32 v250, %17\n v_mov_b32 v251, %18\n v_mov_b32 v252, %19\n v_mov_b32 v240, %20\n v_mov_b32 v241, %21\n" \
  "s_mov_b32 s59, %25\n s_lshl_b32 s61, %26, 8\n s_lshl_b32 s62, %27, 8\n s_lshl_b32 s63, %28, 8\n" \
  "v_mov_b32 v253, %22\n v_mov_b32 v254, %23\n v_mov_b32 v255, %24\n s_waitcnt vmcnt(0) lgkmcnt(0)\n"
#define RH_OPS \
  :: "s"(c.p.ws), "s"(c.p.wq), "s"(c.p.wk), "s"(c.p.wv), "s"(h), "s"(wave), "s"(c.ring), "s"(dst), "s"(wb), "s"(fn), "s"(c.dbg), \
     "v"(vl), "v"(vwa), "v"(sidx), "v"(img), "v"(vvx), "v"(vo), "v"(vvm), "v"(prd), "v"(pmr), "v"(vw), "v"(tp), "v"(vkx), "v"(trq), "v"(pmw), \
     "s"(c.p.dt), "s"(c.p.d2), "s"(c.p.de), "s"(c.p.dp) \
  : "memory", "s40", "s41", "s42", "s43", "s44", "s45", "s46", "s47", "s48", "s49", "s50", "s51", "s52", "s53", "s54", "s55", "s56", "s57", "s58", "s59", "s61", "s62", "s63", \
    "s78", "s79", "s80", "s81", "s99", "v240", "v241", "v244", "v245", "v246", "v247", "v248", "v249", "v250", "v251", "v252", "v253", "v254", "v255"
  if constexpr (AW) asm volatile(RH_PRO RH_ASM_A RH_OPS); else asm volatile(RH_PRO RH_ASM_N RH_OPS);
}

__device__ __forceinline__ void rw_role(const Ctx& c, int idx) {
  const int rg = idx & 1, col = idx >> 1, wave = c.wave, lane = c.lane, g = lane >> 4, cc = lane & 15, q = (lane >> 2) & 3;
  const unsigned vl = lane * 16, sidx = (lane & 4) ? 0xEEEE : 0x4444, vh = (wave * 4 + (lane & 3)) * 128, rwa = wave * 512 + ((g >> 1) * 2 + (cc >> 3)) * 128 + (g & 1) * 64 + (cc & 7) * 4;
  const unsigned vp = lane * 4, vw = (lane >> 2) * 1280 + (lane & 3) * 16;
  const unsigned tp = wave * 16384 + (16 * (g >> 1) + 8 * (q >> 1) + 4 * (g & 1) + (q & 1)) * 64 + (lane & 3) * 8;
  const unsigned dst = wave * 16384, wb = wave * 512 * 1280 + col * 64;
  const unsigned fn = c.ring + (rg * 4 + (wave >> 1)) * 16384 + (wave & 1) * 4096, mo = c.ring + (8 + (col >> 4)) * 16384 + (col & 15) * 512 + rg * 256;
  RH_SETS();
  asm volatile(
    "s_mov_b64 s[40:41], %0\n s_mov_b32 s42, RH_WSB\n s_mov_b32 s43, 0x20000\n"
    "s_mov_b64 s[44:45], %1\n s_mov_b32 s46, 0x500000\n s_mov_b32 s47, 0x20000\n"
    "s_and_b32 s41, s41, 0xffff\n s_and_b32 s45, s45, 0xffff\n"
    "s_mov_b32 s57, %2\n s_mov_b32 s58, %3\n s_mov_b32 s78, %4\n s_mov_b32 s79, %5\n s_mov_b32 s80, %6\n s_mov_b32 s81, %7\n s_mov_b32 s62, %8\n"
    "v_mov_b32 v244, %9\n v_mov_b32 v246, %10\n v_mov_b32 v247, %11\n v_mov_b32 v248, %12\n v_mov_b32 v249, %13\n v_mov_b32 v240, %14\n"
    "v_mov_b32 v241, %15\n s_waitcnt vmcnt(0) lgkmcnt(0)\n"
    RW_ASM
    :: "s"(c.p.ws), "s"(c.p.wo), "s"(wave), "s"(c.ring), "s"(dst), "s"(wb), "s"(fn), "s"(c.dbg), "s"(mo),
       "v"(vl), "v"(sidx), "v"(vh), "v"(rwa), "v"(vp), "v"(vw), "v"(tp)
    : "memory", "s40", "s41", "s42", "s43", "s44", "s45", "s46", "s47", "s57", "s58", "s62", "s78", "s79", "s80", "s81", "s99",
      "v240", "v241", "v244", "v246", "v247", "v248", "v249");
}

__device__ __forceinline__ int role_of(int x, int i) {
  if (x < 4) {
    if (i < 8) return R_RH << 8 | (x * 8 + i);
    if (i < 18) return R_RW << 8 | (((x >> 1) * 10 + i - 8) * 2 + (x & 1));
    if (i < 22) return R_ATT << 8 | ((i & 1) * 8 + x * 2 + ((i - 18) >> 1));
    return R_QKV << 8 | ((i & 1) * 48 + x * 8 + ((i - 22) >> 1));
  }
  const int y = x - 4;
  if (i < 2) return R_RED << 8 | ((i & 1) * 4 + y);
  if (i < 10) return R_WO << 8 | ((i & 1) * 16 + y * 4 + ((i - 2) >> 1));
  if (i < 16) return R_QKV << 8 | ((i & 1) * 48 + y * 8 + 5 + ((i - 10) >> 1));
  if (i < 24) return R_QKV << 8 | ((i & 1) * 48 + 32 + y * 4 + ((i - 16) >> 1));
  const int q = (i - 24) >> 1;
  return (y < 3 && q < 3) ? (R_F2 << 8 | ((i & 1) * 9 + y * 3 + q)) : (R_F1 << 8 | ((i & 1) * 7 + (y < 3 ? y : 3 + q)));
}
__device__ __forceinline__ unsigned ring_base(const char* ws) {
  const uintptr_t a = (uintptr_t)ws; return (unsigned)(((a + FR_MIN + (2u << 20) - 1) & ~(uintptr_t)((2u << 20) - 1)) - a);
}

__global__ void __launch_bounds__(512, 2) main_kernel(Params p) {
  extern __shared__ __attribute__((aligned(16))) char smem[];
  Ctx c; c.p = p; c.ws = mkrsrc(p.ws, WS_END); c.lane = threadIdx.x & 63; c.wave = UNI(threadIdx.x >> 6);
  c.lds = (ldsp)smem; c.lds0 = (unsigned)(uintptr_t)c.lds; c.dbg = 0;
  const unsigned calw = ld4(c.ws, c.lane * 4, CT_CAL);
  if (threadIdx.x == 0) {
    unsigned* ct = (unsigned*)p.ws;
    const unsigned x = xcc_id();
    const unsigned t = __hip_atomic_fetch_add(ct + x * 32, 1u, __ATOMIC_RELAXED, __HIP_MEMORY_SCOPE_AGENT);
    int role = R_IDLE;
    if (t < 32) role = role_of(x, t);
    else {
      unsigned o = __hip_atomic_fetch_add(ct + CT_OVF / 4, 1u, __ATOMIC_RELAXED, __HIP_MEMORY_SCOPE_AGENT);
      for (unsigned spins = 0; spins < SPIN_CAP; ++spins) {
        unsigned n = 0;
        for (int y = 0; y < 8; ++y) n += __hip_atomic_load(ct + y * 32, __ATOMIC_RELAXED, __HIP_MEMORY_SCOPE_AGENT);
        if (n >= 256u) break;
      }
      for (int y = 0; y < 8; ++y) {
        const unsigned n = __hip_atomic_load(ct + y * 32, __ATOMIC_RELAXED, __HIP_MEMORY_SCOPE_AGENT);
        if (n < 32) { if (o < 32 - n) { role = role_of(y, n + o); break; } o -= 32 - n; }
      }
    }
    *(LAS int*)c.lds = role; *(LAS int*)(c.lds + 64) = 0; *(LAS int*)(c.lds + 128) = 0; *(LAS v4u*)(c.lds + 1024 + 192) = (v4u){0u, 0u, 0u, 0u}; *(LAS v2u*)(c.lds + 224) = (v2u){0u, 0u}; *(LAS v2u*)(c.lds + 1024 + 224) = (v2u){0u, 0u}; *(LAS int*)(c.lds + 1088) = 0; *(LAS int*)(c.lds + 1152) = 0;
  }
  __syncthreads();
  const int role = UNI(*(LAS int*)c.lds);
  c.fb = ring_base(p.ws);
  const unsigned long long cal = __builtin_amdgcn_ballot_w64(calw & 1u);
  c.cm = (unsigned)(cal >> 1); c.ring = c.fb + ((unsigned)cal & 1u) * 8192u; c.ringb = c.ring ^ 8192u;
  BARRIER();
  const int idx = role & 255;
  int ty = role >> 8;
  if (ty == R_WO || ty == R_F1 || ty == R_F2) for (int i = 0; i < LATE; ++i) __builtin_amdgcn_s_sleep(32);
#define RUN(cond, call) if (cond) { asm volatile("" : "+v"(c.lane)); __builtin_amdgcn_s_waitcnt(0); call; } else
  RUN(ty == R_RH && c.wave == 7, rh_wave<true>(c, idx))
  RUN(ty == R_RH, rh_wave<false>(c, idx))
  RUN(ty == R_RW, rw_role(c, idx))
  RUN(ty == R_QKV && idx % 48 < 32 && c.wave >= 4, (gemm_role<R_QKV, true>(c, idx)))
  RUN(ty == R_QKV, (gemm_role<R_QKV, false>(c, idx)))
  RUN(ty == R_ATT, att_role(c, idx))
  RUN(ty == R_WO, (gemm_role<R_WO, false>(c, idx)))
  RUN(ty == R_F1, (gemm_role<R_F1, false>(c, idx)))
  RUN(ty == R_F2, (gemm_role<R_F2, false>(c, idx)))
  RUN(ty == R_RED, red_role(c, idx)) {}
}

__global__ void __launch_bounds__(576) init_kernel(Params p) {
  __shared__ __attribute__((aligned(16))) char img[16384];
  const int b = blockIdx.x, tid = threadIdx.x, lane = tid & 63, wave = UNI(tid >> 6);
  const unsigned ring = ring_base(p.ws);
  if (wave == 8) {
    auto slot_of = [](int ch, int s) { return ch ? (s + LAG < 63 ? s + LAG : 63) + 1 + s : s + (s > LAG ? s - LAG : 0); };
    unsigned* const tab = (unsigned*)(p.ws + CT_TAB);
    if (b < 33) {
      const rsrc_t ws = mkrsrc(p.ws, WS_END);
      unsigned lat[2] = {~0u, ~0u};
      st4(ws, lane < 2 ? lane * 8192u : 0x80000000u, ring + ((unsigned)b << 21) + RB_CAL * BK, 0u); WAITV(0);
      for (int rep = 0; rep < 4; ++rep) {
        const unsigned off = ring + ((unsigned)b << 21) + RB_CAL * BK + (rep & 1) * 8192;
        const unsigned long long t0 = __builtin_amdgcn_s_memtime();
        st4(ws, lane ? 0x80000000u : 0u, off, 0u); WAITV(0);
        const unsigned d = (unsigned)(__builtin_amdgcn_s_memtime() - t0);
        if (rep & 1) lat[1] = umin(lat[1], d); else lat[0] = umin(lat[0], d);
      }
      const unsigned cb = (lat[1] < lat[0] ? 1u : 0u) ^ (xcc_id() >> 2);
      if (lane == 0) *(unsigned*)(p.ws + CT_CAL + b * 4) = cb;
      if (b > 0 && lane < 4) tab[slot_of((b - 1) >> 4, ((b - 1) & 15) + 16 * lane) * 4] = ring + ((unsigned)b << 21) + cb * 8192;
    } else if (b < 35) {
      const int ch = b - 33, s = lane, u = slot_of(ch, s);
      const bool same = ch ? s + LAG + 1 >= NSEG : s < LAG;
      tab[u * 4 + 1] = (ch * 4 + (s & 3)) * 10 * BK; tab[u * 4 + 2] = (ch * 4 + ((s + 2) & 3)) * 10 * BK;
      tab[u * 4 + 3] = (s + 1) | ch << 12 | (same ? 1 << 16 : 0) | (u == 127 ? 1 << 17 : 0);
      if (lane < 16) tab[512 + lane] = 0u;
    } else if (b < 37) *(v4u*)(p.ws + (b - 35) * 1024 + lane * 16) = (v4u){0u, 0u, 0u, 0u};
    return;
  }
  const rsrc_t xr = mkrsrc(p.x + (size_t)(b * 8 + wave) * DM, 1280);
  const v4u xa = __builtin_amdgcn_raw_buffer_load_b128(xr, lane * 16, 0, 0), xb = __builtin_amdgcn_raw_buffer_load_b128(xr, lane * 16 + 1024, 0, 0);
  for (unsigned o = (b * 512 + tid) * 16; o < RB_N * BK; o += 256 * 512 * 16) {
    const int blk = o >> 14; const unsigned w = o & 8191;
    v4u v = {SENT, SENT, SENT, SENT};
    if (blk == RB_CAL) continue;
    if (blk >= RB_HINT) v = (v4u){0u, 0u, 0u, 0u};
    else if (blk % 40 == 8 || (blk % 40 == 9 && w < 2048)) {
      const unsigned mo = (blk % 40 - 8) * 8192 + w;
      v = *(const v4u*)(p.mem0 + (((mo >> 8) & 1) * 4 + ((mo >> 4) & 3)) * DM + (mo >> 9) * 32 + ((mo >> 6) & 3) * 8);
#pragma unroll
      for (int j = 0; j < 4; ++j) v[j] = nosent(v[j]);
    }
    *(v4u*)(p.ws + ring + o) = v;
  }
  {
    auto wsum = [](float v) {
      v += __int_as_float(__builtin_amdgcn_update_dpp(0, __float_as_int(v), 0x121, 0xf, 0xf, true));
      v += __int_as_float(__builtin_amdgcn_update_dpp(0, __float_as_int(v), 0x122, 0xf, 0xf, true));
      v += __int_as_float(__builtin_amdgcn_update_dpp(0, __float_as_int(v), 0x124, 0xf, 0xf, true));
      v += ror8(v); return sw32add(sw16add(v)); };
    float s1 = 0.f, s2 = 0.f;
#pragma unroll
    for (int i = 0; i < 4; ++i) s1 += (blo(xa[i]) + bhi(xa[i])) + (blo(xb[i]) + bhi(xb[i]));
    const float mean = wsum(s1) * (1.0f / 640.0f), mb = lane < 16 ? mean : 0.f;
#pragma unroll
    for (int i = 0; i < 4; ++i) { const float a = blo(xa[i]) - mean, b = bhi(xa[i]) - mean, d = blo(xb[i]) - mb, e = bhi(xb[i]) - mb; s2 += (a * a + b * b) + (d * d + e * e); }
    const float rs = __builtin_amdgcn_rsqf(wsum(s2) * (1.0f / 640.0f) + 1e-5f), nb = -mean * rs;
    auto nrm = [&](unsigned u) { return pk2(fmaf(blo(u), rs, nb), fmaf(bhi(u), rs, nb)); };
    const ldsp w = (ldsp)img + (lane >> 1) * 256 + wave * 16 + (lane & 1) * 8;
    *(LAS v2u*)w = (v2u){nrm(xa.x), nrm(xa.y)}; *(LAS v2u*)(w + 128) = (v2u){nrm(xa.z), nrm(xa.w)};
    *(LAS v2u*)(w + 8192) = (v2u){nrm(xb.x), nrm(xb.y)}; *(LAS v2u*)(w + 8192 + 128) = (v2u){nrm(xb.z), nrm(xb.w)};
  }
  BARRIER();
  {
    const unsigned tg = b & 3, ch = tid >> 3;
    char* o = p.ws + XN1_O + (b >> 2) * T40 + ch * 512 + ((tg ^ ((ch >> 2) & 3)) * 8 + (tid & 7)) * 16;
    *(v4u*)o = lds16((ldsp)img + tid * 16);
    if (tid < 128) *(v4u*)(o + 32768) = lds16((ldsp)img + 8192 + tid * 16);
  }
}

extern "C" void kernel_launch(void* const* d_in, const int* in_sizes, int n_in, void* d_out, int out_size, void* d_ws, size_t ws_size, hipStream_t stream) {
  Params p;
  p.x = (const unsigned short*)d_in[0]; p.mem0 = (const unsigned short*)d_in[1]; p.wq = (const unsigned short*)d_in[2]; p.wk = (const unsigned short*)d_in[3];
  p.wv = (const unsigned short*)d_in[4]; p.wo = (const unsigned short*)d_in[5]; p.w1 = (const unsigned short*)d_in[8]; p.w2 = (const unsigned short*)d_in[9];
  p.out = (unsigned short*)d_out; p.ws = (char*)d_ws;
  p.dt = HINT_DT; p.d2 = HINT_D2; p.de = HINT_DE; p.dp = HINT_DP;
  (void)hipFuncSetAttribute((const void*)main_kernel, hipFuncAttributeMaxDynamicSharedMemorySize, LDS_BYTES);
  init_kernel<<<256, 576, 0, stream>>>(p);
  main_kernel<<<256, 512, LDS_BYTES, stream>>>(p);
}
```
